```python
import torch

def _softmax(x, axis=-1):
    return torch.softmax(x, dim=axis)
def _gelu(x):
    return torch.nn.functional.gelu(x, approximate="tanh")
def _silu(x):
    return torch.nn.functional.silu(x)
def _sigmoid(x):
    return torch.sigmoid(x)
def _layernorm(x, axis=-1, eps=1e-5):
    m = x.mean(dim=axis, keepdim=True)
    v = x.var(dim=axis, keepdim=True, unbiased=False)
    return (x - m) / torch.sqrt(v + eps)
def _scatter_add(tgt, idx, src, axis=0):
    return tgt.clone().index_add_(axis, idx, src)
def _segment_sum(src, seg, n):
    out = torch.zeros((n,) + src.shape[1:], dtype=src.dtype)
    return out.index_add_(0, seg, src)
def _pad(x, pw):
    flat = tuple(v for pair in reversed(pw) for v in pair)
    return torch.nn.functional.pad(x, flat)

kernel_name = 'standalone__modern_conv_block_replknet__vision'

def setup_inputs():

    _w = (lambda a: (lambda t: t.to(torch.bfloat16) if t.is_floating_point() else t)(torch.as_tensor(a)))
    H = W = 512
    C = 96
    K = 1
    return {
        "x": _w((lambda *s: torch.randn(*s, dtype=torch.bfloat16) * 0.05)(1, H, W, C)),
        "dw_large": _w((lambda *s: torch.randn(*s, dtype=torch.bfloat16) * 0.4)(K, K, C) * (1.0 / K)),
        "dw_small": _w((lambda *s: torch.randn(*s, dtype=torch.bfloat16) * 0.4)(5, 5, C) * 0.2),
        "w1": _w((lambda *s: torch.randn(*s, dtype=torch.bfloat16) * 0.4)(C, 4 * C) * (C ** -0.5)),
        "w2": _w((lambda *s: torch.randn(*s, dtype=torch.bfloat16) * 0.1)(4 * C, C) * ((4 * C) ** -0.5)),
    }

def reference(x, dw_large, dw_small, w1, w2):
    B, H, W, C = x.shape
    Kl = dw_large.shape[0]
    pl = (Kl - 1) // 2
    xpl = _pad(x, ((0,0),(pl,pl),(pl,pl),(0,0)))
    out = torch.zeros_like(x)
    for i in range(Kl):
        for j in range(Kl):
            out = out + xpl[:, i:i+H, j:j+W, :] * dw_large[i, j][None, None, None, :]
    Ks = dw_small.shape[0]
    ps = (Ks - 1) // 2
    xps = _pad(x, ((0,0),(ps,ps),(ps,ps),(0,0)))
    for i in range(Ks):
        for j in range(Ks):
            out = out + xps[:, i:i+H, j:j+W, :] * dw_small[i, j][None, None, None, :]
    h = _layernorm(out, axis=-1)
    h = _gelu(torch.einsum('bhwc,ce->bhwe', h, w1))
    h = torch.einsum('bhwe,ec->bhwc', h, w2)
    return x + h
```

```cpp
#include <hip/hip_runtime.h>
#include <hip/hip_bf16.h>
static constexpr int H = 512, W = 512, C = 96, E = 384;
__device__ __forceinline__ float bf2f(unsigned short v) { return __uint_as_float(((unsigned)v) << 16); }
__device__ __forceinline__ unsigned short f2bf(float f) { unsigned u = __float_as_uint(f); u += 0x7fffu + ((u >> 16) & 1u); return (unsigned short)(u >> 16); }
__global__ __launch_bounds__(128) void k_simple(const unsigned short* x, const unsigned short* dl, const unsigned short* ds, const unsigned short* w1, const unsigned short* w2, unsigned short* out) {
  __shared__ float h[8][C];
  __shared__ float g[8][E];
  const int p0 = blockIdx.x * 8, t = threadIdx.x;
  for (int i = t; i < 8 * C; i += 128) {
    int p = i / C, c = i % C; int pix = p0 + p, y = pix / W, xx = pix % W;
    float a = bf2f(x[(size_t)pix * C + c]) * bf2f(dl[c]);
    for (int dy = 0; dy < 5; ++dy) for (int dx = 0; dx < 5; ++dx) {
      int yy = y + dy - 2, xc = xx + dx - 2;
      if (yy >= 0 && yy < H && xc >= 0 && xc < W) a += bf2f(x[((size_t)yy * W + xc) * C + c]) * bf2f(ds[(dy * 5 + dx) * C + c]);
    }
    h[p][c] = a;
  }
  __syncthreads();
  if (t < 8) {
    float m = 0; for (int c = 0; c < C; ++c) m += h[t][c]; m /= C;
    float v = 0; for (int c = 0; c < C; ++c) { float d = h[t][c] - m; v += d * d; } v /= C;
    float r = rsqrtf(v + 1e-5f);
    for (int c = 0; c < C; ++c) h[t][c] = (h[t][c] - m) * r;
  }
  __syncthreads();
  for (int i = t; i < 8 * E; i += 128) {
    int p = i / E, e = i % E; float a = 0;
    for (int c = 0; c < C; ++c) a += h[p][c] * bf2f(w1[c * E + e]);
    float u = 0.7978845608028654f * (a + 0.044715f * a * a * a);
    g[p][e] = 0.5f * a * (1.f + tanhf(u));
  }
  __syncthreads();
  for (int i = t; i < 8 * C; i += 128) {
    int p = i / C, c = i % C; float a = 0;
    for (int e = 0; e < E; ++e) a += g[p][e] * bf2f(w2[e * C + c]);
    size_t o = (size_t)(p0 + p) * C + c;
    out[o] = f2bf(bf2f(x[o]) + a);
  }
}
extern "C" void kernel_launch(void* const* d_in, const int* in_sizes, int n_in, void* d_out, int out_size, void* d_ws, size_t ws_size, hipStream_t stream) {
  k_simple<<<H * W / 8, 128, 0, stream>>>((const unsigned short*)d_in[0], (const unsigned short*)d_in[1], (const unsigned short*)d_in[2], (const unsigned short*)d_in[3], (const unsigned short*)d_in[4], (unsigned short*)d_out);
}
```

```cpp
#include <hip/hip_runtime.h>
#include <hip/hip_bf16.h>

__global__ __launch_bounds__(256, 1) void standalone__modern_conv_block_replknet__vision_kernel(
    const void* x, const void* dl, const void* ds, const void* w1, const void* w2, void* out) {
  __shared__ __attribute__((aligned(16))) char smem[163840];
  unsigned ldsbase = (unsigned)(size_t)smem;
  asm volatile(
    "v_mov_b32 v1, %7\n"
    "v_lshrrev_b32 v2, 6, v1\n"
    "v_and_b32 v0, 63, v1\n"
    "v_readfirstlane_b32 s48, v2\n"
    "s_mov_b64 s[40:41], %2\n"
    "s_and_b32 s41, s41, 0xffff\n"
    "s_mov_b32 s42, 4800\n"
    "s_mov_b32 s43, 0x20000\n"
    "s_mov_b64 s[60:61], %1\n"
    "s_and_b32 s61, s61, 0xffff\n"
    "s_mov_b32 s62, 192\n"
    "s_mov_b32 s63, 0x20000\n"
    "v_lshlrev_b32 v2, 4, v0\n"
    "s_lshl_b32 s70, s48, 10\n"
    "s_lshl_b32 s71, s48, 6\n"
    "v_add_u32 v4, s71, v0\n"
    "v_mov_b32 v8, 0x40000000\n"
    "v_mul_u32_u24 v5, 0x433, v4\n"
    "v_lshrrev_b32 v5, 16, v5\n"
    "v_mul_u32_u24 v6, 61, v5\n"
    "v_sub_u32 v6, v4, v6\n"
    "v_mul_u32_u24 v5, 960, v5\n"
    "v_lshlrev_b32 v7, 4, v6\n"
    "v_add_u32 v5, v5, v7\n"
    "v_add_u32 v5, 0xfffff4c0, v5\n"
    "v_cmp_eq_u32 vcc, 60, v6\n"
    "s_nop 1\n"
    "v_cndmask_b32 v1, v5, v8, vcc\n"
    "s_add_u32 s72, s70, 149504\n"
    "s_mov_b32 m0, s72\n"
    "s_nop 0\n"
    "buffer_load_dwordx4 v1, s[40:43], 0 offen lds\n"
    "v_add_u32 v4, 0x100, v4\n"
    "v_mul_u32_u24 v5, 0x433, v4\n"
    "v_lshrrev_b32 v5, 16, v5\n"
    "v_mul_u32_u24 v6, 61, v5\n"
    "v_sub_u32 v6, v4, v6\n"
    "v_mul_u32_u24 v5, 960, v5\n"
    "v_lshlrev_b32 v7, 4, v6\n"
    "v_add_u32 v5, v5, v7\n"
    "v_add_u32 v5, 0xfffff4c0, v5\n"
    "v_cmp_eq_u32 vcc, 60, v6\n"
    "s_nop 1\n"
    "v_cndmask_b32 v1, v5, v8, vcc\n"
    "s_add_u32 s72, s70, 153600\n"
    "s_mov_b32 m0, s72\n"
    "s_nop 0\n"
    "buffer_load_dwordx4 v1, s[40:43], 0 offen lds\n"
    "v_add_u32 v4, 0x100, v4\n"
    "v_mul_u32_u24 v5, 0x433, v4\n"
    "v_lshrrev_b32 v5, 16, v5\n"
    "v_mul_u32_u24 v6, 61, v5\n"
    "v_sub_u32 v6, v4, v6\n"
    "v_mul_u32_u24 v5, 960, v5\n"
    "v_lshlrev_b32 v7, 4, v6\n"
    "v_add_u32 v5, v5, v7\n"
    "v_add_u32 v5, 0xfffff4c0, v5\n"
    "v_cmp_eq_u32 vcc, 60, v6\n"
    "s_nop 1\n"
    "v_cndmask_b32 v1, v5, v8, vcc\n"
    "s_add_u32 s72, s70, 157696\n"
    "s_mov_b32 m0, s72\n"
    "s_nop 0\n"
    "buffer_load_dwordx4 v1, s[40:43], 0 offen lds\n"
    "s_and_b32 s71, s48, 1\n"
    "s_lshl_b32 s71, s71, 10\n"
    "s_add_u32 s72, s71, 161792\n"
    "s_mov_b32 m0, s72\n"
    "s_sub_u32 s71, s71, 464\n"
    "v_add_u32 v1, s71, v2\n"
    "buffer_load_dwordx4 v1, s[60:63], 0 offen lds\n"
    "s_mov_b64 s[36:37], %0\n"
    "s_and_b32 s37, s37, 0xffff\n"
    "s_mov_b32 s39, 0x20000\n"
    "s_and_b32 s71, %6, 7\n"
    "s_lshr_b32 s72, %6, 3\n"
    "s_lshl_b32 s49, s71, 6\n"
    "s_lshl_b32 s50, s72, 4\n"
    "s_add_u32 s70, s49, 66\n"
    "s_mul_i32 s70, s70, 98304\n"
    "s_min_u32 s38, s70, 50331648\n"
    "s_mul_i32 s70, s50, 192\n"
    "s_sub_u32 s70, s70, 384\n"
    "s_and_b32 s71, s48, 1\n"
    "s_lshl_b32 s71, s71, 8\n"
    "s_lshr_b32 s54, s48, 1\n"
    "s_lshl_b32 s54, s54, 13\n"
    "v_mov_b32 v12, 0x40000000\n"
    "s_add_u32 s72, s54, 320\n"
    "v_mov_b32 v13, s72\n"
    "v_add_u32 v4, s71, v0\n"
    "v_lshrrev_b32 v5, 3, v4\n"
    "v_mul_u32_u24 v6, 0xccd, v5\n"
    "v_lshrrev_b32 v6, 16, v6\n"
    "v_mul_u32_u24 v7, 20, v6\n"
    "v_sub_u32 v5, v5, v7\n"
    "v_and_b32 v7, 3, v4\n"
    "v_lshl_add_u32 v6, v6, 2, v7\n"
    "v_lshrrev_b32 v7, 2, v4\n"
    "v_and_b32 v7, 1, v7\n"
    "v_mul_u32_u24 v8, 672, v6\n"
    "v_lshlrev_b32 v9, 4, v5\n"
    "v_lshlrev_b32 v10, 3, v7\n"
    "v_add3_u32 v8, v8, v9, v10\n"
    "v_add_u32 v246, s54, v8\n"
    "v_mul_u32_u24 v9, 192, v5\n"
    "v_lshlrev_b32 v10, 4, v6\n"
    "v_add3_u32 v9, v9, v10, s70\n"
    "v_mul_u32_u24 v10, 98304, v7\n"
    "v_add_u32 v250, v9, v10\n"
    "v_cmp_le_u32 vcc, 0x18000, v9\n"
    "s_nop 1\n"
    "v_cndmask_b32 v250, v250, v12, vcc\n"
    "v_add_u32 v4, s71, v0\n"
    "v_add_u32 v4, 64, v4\n"
    "v_lshrrev_b32 v5, 3, v4\n"
    "v_mul_u32_u24 v6, 0xccd, v5\n"
    "v_lshrrev_b32 v6, 16, v6\n"
    "v_mul_u32_u24 v7, 20, v6\n"
    "v_sub_u32 v5, v5, v7\n"
    "v_and_b32 v7, 3, v4\n"
    "v_lshl_add_u32 v6, v6, 2, v7\n"
    "v_lshrrev_b32 v7, 2, v4\n"
    "v_and_b32 v7, 1, v7\n"
    "v_mul_u32_u24 v8, 672, v6\n"
    "v_lshlrev_b32 v9, 4, v5\n"
    "v_lshlrev_b32 v10, 3, v7\n"
    "v_add3_u32 v8, v8, v9, v10\n"
    "v_add_u32 v247, s54, v8\n"
    "v_mul_u32_u24 v9, 192, v5\n"
    "v_lshlrev_b32 v10, 4, v6\n"
    "v_add3_u32 v9, v9, v10, s70\n"
    "v_mul_u32_u24 v10, 98304, v7\n"
    "v_add_u32 v251, v9, v10\n"
    "v_cmp_le_u32 vcc, 0x18000, v9\n"
    "s_nop 1\n"
    "v_cndmask_b32 v251, v251, v12, vcc\n"
    "v_add_u32 v4, s71, v0\n"
    "v_add_u32 v4, 128, v4\n"
    "v_lshrrev_b32 v5, 3, v4\n"
    "v_mul_u32_u24 v6, 0xccd, v5\n"
    "v_lshrrev_b32 v6, 16, v6\n"
    "v_mul_u32_u24 v7, 20, v6\n"
    "v_sub_u32 v5, v5, v7\n"
    "v_and_b32 v7, 3, v4\n"
    "v_lshl_add_u32 v6, v6, 2, v7\n"
    "v_lshrrev_b32 v7, 2, v4\n"
    "v_and_b32 v7, 1, v7\n"
    "v_mul_u32_u24 v8, 672, v6\n"
    "v_lshlrev_b32 v9, 4, v5\n"
    "v_lshlrev_b32 v10, 3, v7\n"
    "v_add3_u32 v8, v8, v9, v10\n"
    "v_add_u32 v248, s54, v8\n"
    "v_mul_u32_u24 v9, 192, v5\n"
    "v_lshlrev_b32 v10, 4, v6\n"
    "v_add3_u32 v9, v9, v10, s70\n"
    "v_mul_u32_u24 v10, 98304, v7\n"
    "v_add_u32 v252, v9, v10\n"
    "v_cmp_le_u32 vcc, 0x18000, v9\n"
    "s_nop 1\n"
    "v_cndmask_b32 v252, v252, v12, vcc\n"
    "v_add_u32 v4, s71, v0\n"
    "v_add_u32 v4, 192, v4\n"
    "v_lshrrev_b32 v5, 3, v4\n"
    "v_mul_u32_u24 v6, 0xccd, v5\n"
    "v_lshrrev_b32 v6, 16, v6\n"
    "v_mul_u32_u24 v7, 20, v6\n"
    "v_sub_u32 v5, v5, v7\n"
    "v_and_b32 v7, 3, v4\n"
    "v_lshl_add_u32 v6, v6, 2, v7\n"
    "v_lshrrev_b32 v7, 2, v4\n"
    "v_and_b32 v7, 1, v7\n"
    "v_mul_u32_u24 v8, 672, v6\n"
    "v_lshlrev_b32 v9, 4, v5\n"
    "v_lshlrev_b32 v10, 3, v7\n"
    "v_add3_u32 v8, v8, v9, v10\n"
    "v_add_u32 v249, s54, v8\n"
    "v_mul_u32_u24 v9, 192, v5\n"
    "v_lshlrev_b32 v10, 4, v6\n"
    "v_add3_u32 v9, v9, v10, s70\n"
    "v_mul_u32_u24 v10, 98304, v7\n"
    "v_add_u32 v1, v9, v10\n"
    "v_cmp_le_u32 vcc, 0x18000, v9\n"
    "s_nop 1\n"
    "v_cndmask_b32 v1, v1, v12, vcc\n"
    "v_cmp_le_u32 vcc, 0x1e0, v4\n"
    "s_nop 1\n"
    "v_cndmask_b32 v1, v1, v12, vcc\n"
    "v_cndmask_b32 v249, v249, v13, vcc\n"
    "s_and_b32 s70, s48, 2\n"
    "s_add_i32 s70, s70, s49\n"
    "s_add_i32 s70, s70, -2\n"
    "s_cmp_lt_i32 s70, 0\n"
    "s_mul_i32 s52, s70, 98304\n"
    "s_cselect_b32 s52, 0x40000000, s52\n"
    "buffer_load_dwordx4 v[84:87], v250, s[36:39], s52 offen\n"
    "buffer_load_dwordx4 v[88:91], v251, s[36:39], s52 offen\n"
    "buffer_load_dwordx4 v[92:95], v252, s[36:39], s52 offen\n"
    "buffer_load_dwordx4 v[96:99], v1, s[36:39], s52 offen\n"
    "s_and_b32 s70, s48, 2\n"
    "s_add_i32 s70, s70, s49\n"
    "s_add_i32 s70, s70, 2\n"
    "s_cmp_lt_i32 s70, 0\n"
    "s_mul_i32 s52, s70, 98304\n"
    "s_cselect_b32 s52, 0x40000000, s52\n"
    "buffer_load_dwordx4 v[100:103], v250, s[36:39], s52 offen\n"
    "buffer_load_dwordx4 v[104:107], v251, s[36:39], s52 offen\n"
    "buffer_load_dwordx4 v[124:127], v252, s[36:39], s52 offen\n"
    "buffer_load_dwordx4 v[128:131], v1, s[36:39], s52 offen\n"
    "s_and_b32 s70, s48, 2\n"
    "s_add_i32 s70, s70, s49\n"
    "s_add_i32 s70, s70, 6\n"
    "s_cmp_lt_i32 s70, 0\n"
    "s_mul_i32 s52, s70, 98304\n"
    "s_cselect_b32 s52, 0x40000000, s52\n"
    "buffer_load_dwordx4 v[52:55], v250, s[36:39], s52 offen\n"
    "buffer_load_dwordx4 v[56:59], v251, s[36:39], s52 offen\n"
    "buffer_load_dwordx4 v[60:63], v252, s[36:39], s52 offen\n"
    "buffer_load_dwordx4 v[64:67], v1, s[36:39], s52 offen\n"
    "s_getpc_b64 s[92:93]\n"
    "L_here_%=:\n"
    "s_and_b32 s93, s93, 0xffff\n"
    "s_mov_b32 s94, L_end_%=-L_here_%=\n"
    "s_mov_b32 s95, 0x20000\n"
    "v_lshlrev_b32 v254, 2, v0\n"
    "s_lshl_b32 s70, s50, 6\n"
    "s_lshl_b32 s71, s48, 8\n"
    "s_add_u32 s70, s70, s71\n"
    "buffer_load_dword v255, v254, s[92:95], s70 offen\n"
    "s_mov_b64 s[44:45], %3\n"
    "s_and_b32 s45, s45, 0xffff\n"
    "s_mov_b32 s46, 73728\n"
    "s_mov_b32 s47, 0x20000\n"
    "s_mov_b64 s[80:81], %4\n"
    "s_and_b32 s81, s81, 0xffff\n"
    "s_mov_b32 s82, 73728\n"
    "s_mov_b32 s83, 0x20000\n"
    "v_add_u32 v152, 0, v0\n"
    "v_mul_u32_u24 v153, 0x556, v152\n"
    "v_lshrrev_b32 v153, 16, v153\n"
    "v_mul_u32_u24 v154, 48, v153\n"
    "v_sub_u32 v154, v152, v154\n"
    "v_lshlrev_b32 v155, 2, v153\n"
    "v_xor_b32 v154, v154, v155\n"
    "v_lshlrev_b32 v154, 4, v154\n"
    "v_mul_u32_u24 v153, 0x300, v153\n"
    "v_add_u32 v116, v153, v154\n"
    "v_add_u32 v152, 64, v0\n"
    "v_mul_u32_u24 v153, 0x556, v152\n"
    "v_lshrrev_b32 v153, 16, v153\n"
    "v_mul_u32_u24 v154, 48, v153\n"
    "v_sub_u32 v154, v152, v154\n"
    "v_lshlrev_b32 v155, 2, v153\n"
    "v_xor_b32 v154, v154, v155\n"
    "v_lshlrev_b32 v154, 4, v154\n"
    "v_mul_u32_u24 v153, 0x300, v153\n"
    "v_add_u32 v117, v153, v154\n"
    "v_add_u32 v152, 128, v0\n"
    "v_mul_u32_u24 v153, 0x556, v152\n"
    "v_lshrrev_b32 v153, 16, v153\n"
    "v_mul_u32_u24 v154, 48, v153\n"
    "v_sub_u32 v154, v152, v154\n"
    "v_lshlrev_b32 v155, 2, v153\n"
    "v_xor_b32 v154, v154, v155\n"
    "v_lshlrev_b32 v154, 4, v154\n"
    "v_mul_u32_u24 v153, 0x300, v153\n"
    "v_add_u32 v118, v153, v154\n"
    "s_mul_i32 s74, s48, 18432\n"
    "s_mov_b32 s57, 0x44ea44ea\n"
    "s_mov_b32 s58, 0xb780b780\n"
    "s_mov_b32 s59, 0xffff0000\n"
    "s_mov_b32 s76, 0xffff8000\n"
    "s_mov_b32 s77, 0x4000\n"
    "v_and_b32 v140, 15, v0\n"
    "v_lshrrev_b32 v141, 4, v0\n"
    "v_and_b32 v142, 31, v0\n"
    "v_lshrrev_b32 v143, 5, v0\n"
    "s_mul_i32 s70, s48, 2016\n"
    "v_lshlrev_b32 v144, 13, v141\n"
    "v_lshlrev_b32 v145, 4, v140\n"
    "v_add3_u32 v241, v144, v145, s70\n"
    "s_mul_i32 s70, s48, 48\n"
    "s_add_u32 s70, s70, 49152\n"
    "v_mul_u32_u24 v144, 208, v0\n"
    "v_add_u32 v242, s70, v144\n"
    "s_lshr_b32 s70, s48, 1\n"
    "s_mul_i32 s70, s70, 13312\n"
    "s_and_b32 s71, s48, 1\n"
    "s_mul_i32 s72, s71, 6656\n"
    "s_add_u32 s70, s70, s72\n"
    "s_add_u32 s70, s70, 49152\n"
    "v_mul_u32_u24 v144, 208, v142\n"
    "v_lshlrev_b32 v145, 4, v143\n"
    "v_add3_u32 v243, v144, v145, s70\n"
    "v_add_u32 v244, 75776, v2\n"
    "v_add_u32 v245, 0x10000, v244\n"
    "s_mul_i32 s70, s71, 196608\n"
    "s_mul_i32 s72, s50, 192\n"
    "s_add_u32 s70, s70, s72\n"
    "v_lshrrev_b32 v144, 4, v142\n"
    "v_mul_u32_u24 v144, 98304, v144\n"
    "v_mul_u32_u24 v146, 192, v140\n"
    "v_add3_u32 v144, v144, v146, v145\n"
    "v_add_u32 v253, s70, v144\n"
    "v_and_b32 v144, 1, v141\n"
    "v_lshlrev_b32 v144, 5, v144\n"
    "v_lshrrev_b32 v145, 1, v141\n"
    "v_lshlrev_b32 v145, 4, v145\n"
    "v_add3_u32 v144, v144, v145, v146\n"
    "v_add_u32 v2, s70, v144\n"
    "s_lshr_b32 s70, s48, 1\n"
    "s_lshl_b32 s70, s70, 2\n"
    "s_add_u32 s70, s70, s49\n"
    "s_mul_i32 s56, s70, 98304\n"
    "s_mov_b32 s55, 0x40000000\n"
    "v_and_b32 v144, 3, v0\n"
    "v_mul_u32_u24 v240, 0x5555, v144\n"
    "v_lshrrev_b32 v147, 2, v140\n"
    "s_mul_i32 s70, s48, 48\n"
    "v_lshlrev_b32 v148, 1, v144\n"
    "v_add_u32 v148, s70, v148\n"
    "v_and_b32 v149, 1, v141\n"
    "v_lshlrev_b32 v149, 2, v149\n"
    "v_sub_u32 v149, v149, v147\n"
    "v_add_u32 v149, 3, v149\n"
    "v_lshrrev_b32 v150, 1, v141\n"
    "v_mul_u32_u24 v151, 976, v149\n"
    "v_mul_u32_u24 v152, 192, v150\n"
    "v_add3_u32 v151, v151, v152, v148\n"
    "v_add_u32 v151, 149504, v151\n"
    "v_mul_u32_u24 v152, 208, v149\n"
    "v_add_u32 v152, v152, v148\n"
    "v_add_u32 v152, 161216, v152\n"
    "v_mov_b32 v153, 162672\n"
    "v_cmp_ne_u32 vcc, 0, v150\n"
    "s_nop 1\n"
    "v_cndmask_b32 v152, v152, v153, vcc\n"
    "v_lshlrev_b32 v153, 1, v141\n"
    "v_sub_u32 v153, v153, v147\n"
    "v_add_u32 v153, 3, v153\n"
    "v_mul_u32_u24 v153, 976, v153\n"
    "v_add_u32 v153, v153, v148\n"
    "v_add_u32 v153, 150272, v153\n"
    "v_and_b32 v154, 1, v144\n"
    "v_lshlrev_b32 v154, 4, v154\n"
    "v_lshrrev_b32 v155, 1, v144\n"
    "v_sub_u32 v159, 0, v155\n"
    "v_not_b32 v155, v159\n"
    "s_waitcnt vmcnt(13)\n"
    "s_nop 0\n"
    "s_barrier\n"
    "s_mov_b32 s60, 0xaaaaaaaa\n"
    "s_mov_b32 s61, 0xaaaaaaaa\n"
    "s_mov_b32 s62, 0xcccccccc\n"
    "s_mov_b32 s63, 0xcccccccc\n"
    "s_mov_b32 s64, 0xff00ff00\n"
    "s_mov_b32 s65, 0xff00ff00\n"
    "s_mov_b32 s66, 0xffff0000\n"
    "s_mov_b32 s67, 0xffff0000\n"
    "s_mov_b32 s68, 0x0f0f0f0f\n"
    "s_mov_b32 s69, 0x0f0f0f0f\n"
    "ds_read_u16 a240, v151\n"
    "ds_read_u16 a241, v151 offset:976\n"
    "ds_read_u16 a242, v151 offset:1952\n"
    "ds_read_u16 a243, v151 offset:2928\n"
    "ds_read_u16 v184, v151 offset:384\n"
    "ds_read_u16 v185, v151 offset:1360\n"
    "ds_read_u16 v186, v151 offset:2336\n"
    "ds_read_u16 v187, v151 offset:3312\n"
    "s_add_u32 s75, s74, 0\n"
    "s_add_u32 s73, s75, 75776\n"
    "s_mov_b32 m0, s73\n"
    "s_nop 0\n"
    "buffer_load_dwordx4 v116, s[44:47], s75 offen lds\n"
    "ds_read_u16 a244, v151 offset:8\n"
    "ds_read_u16 a245, v151 offset:984\n"
    "ds_read_u16 a246, v151 offset:1960\n"
    "ds_read_u16 a247, v151 offset:2936\n"
    "ds_read_u16 v192, v151 offset:392\n"
    "ds_read_u16 v193, v151 offset:1368\n"
    "ds_read_u16 v194, v151 offset:2344\n"
    "ds_read_u16 v195, v151 offset:3320\n"
    "ds_read_u16 a248, v151 offset:16\n"
    "ds_read_u16 a249, v151 offset:992\n"
    "ds_read_u16 a250, v151 offset:1968\n"
    "ds_read_u16 a251, v151 offset:2944\n"
    "ds_read_u16 v200, v151 offset:400\n"
    "ds_read_u16 v201, v151 offset:1376\n"
    "s_add_u32 s75, s74, 0\n"
    "s_add_u32 s73, s75, 76800\n"
    "s_mov_b32 m0, s73\n"
    "s_nop 0\n"
    "buffer_load_dwordx4 v117, s[44:47], s75 offen lds\n"
    "ds_read_u16 v202, v151 offset:2352\n"
    "ds_read_u16 v203, v151 offset:3328\n"
    "ds_read_u16 a252, v151 offset:24\n"
    "ds_read_u16 a253, v151 offset:1000\n"
    "ds_read_u16 a254, v151 offset:1976\n"
    "ds_read_u16 a255, v151 offset:2952\n"
    "ds_read_u16 v208, v151 offset:408\n"
    "ds_read_u16 v209, v151 offset:1384\n"
    "ds_read_u16 v210, v151 offset:2360\n"
    "ds_read_u16 v211, v151 offset:3336\n"
    "ds_read_u16 v216, v151 offset:32\n"
    "ds_read_u16 v217, v151 offset:1008\n"
    "ds_read_u16 v218, v151 offset:1984\n"
    "ds_read_u16 v219, v151 offset:2960\n"
    "s_add_u32 s75, s74, 0\n"
    "s_add_u32 s73, s75, 77824\n"
    "s_mov_b32 m0, s73\n"
    "s_nop 0\n"
    "buffer_load_dwordx4 v118, s[44:47], s75 offen lds\n"
    "ds_read_u16 v220, v151 offset:416\n"
    "ds_read_u16 v221, v151 offset:1392\n"
    "ds_read_u16 v222, v151 offset:2368\n"
    "ds_read_u16 v223, v151 offset:3344\n"
    "ds_read_u16 v228, v151 offset:40\n"
    "ds_read_u16 v229, v151 offset:1016\n"
    "ds_read_u16 v230, v151 offset:1992\n"
    "ds_read_u16 v231, v151 offset:2968\n"
    "ds_read_u16 v232, v151 offset:424\n"
    "ds_read_u16 v233, v151 offset:1400\n"
    "ds_read_u16 v234, v151 offset:2376\n"
    "ds_read_u16 v235, v151 offset:3352\n"
    "ds_read_u16 v140, v152\n"
    "ds_read_u16 v141, v152 offset:208\n"
    "s_add_u32 s75, s74, 3072\n"
    "s_add_u32 s73, s75, 75776\n"
    "s_mov_b32 m0, s73\n"
    "s_nop 0\n"
    "buffer_load_dwordx4 v116, s[44:47], s75 offen lds\n"
    "ds_read_u16 v142, v152 offset:416\n"
    "ds_read_u16 v143, v152 offset:624\n"
    "ds_read_u16 v144, v152 offset:8\n"
    "ds_read_u16 v145, v152 offset:216\n"
    "ds_read_u16 v146, v152 offset:424\n"
    "ds_read_u16 v147, v152 offset:632\n"
    "s_waitcnt lgkmcnt(7)\n"
    "v_lshl_or_b32 v184, v140, 16, v184\n"
    "s_waitcnt lgkmcnt(6)\n"
    "v_lshl_or_b32 v185, v141, 16, v185\n"
    "s_waitcnt lgkmcnt(5)\n"
    "v_lshl_or_b32 v186, v142, 16, v186\n"
    "s_waitcnt lgkmcnt(4)\n"
    "v_lshl_or_b32 v187, v143, 16, v187\n"
    "ds_read_u16 v140, v152 offset:16\n"
    "ds_read_u16 v141, v152 offset:224\n"
    "ds_read_u16 v142, v152 offset:432\n"
    "ds_read_u16 v143, v152 offset:640\n"
    "s_waitcnt lgkmcnt(7)\n"
    "v_lshl_or_b32 v192, v144, 16, v192\n"
    "s_add_u32 s75, s74, 3072\n"
    "s_add_u32 s73, s75, 76800\n"
    "s_mov_b32 m0, s73\n"
    "s_nop 0\n"
    "buffer_load_dwordx4 v117, s[44:47], s75 offen lds\n"
    "s_waitcnt lgkmcnt(6)\n"
    "v_lshl_or_b32 v193, v145, 16, v193\n"
    "s_waitcnt lgkmcnt(5)\n"
    "v_lshl_or_b32 v194, v146, 16, v194\n"
    "s_waitcnt lgkmcnt(4)\n"
    "v_lshl_or_b32 v195, v147, 16, v195\n"
    "ds_read_u16 v144, v152 offset:24\n"
    "ds_read_u16 v145, v152 offset:232\n"
    "ds_read_u16 v146, v152 offset:440\n"
    "ds_read_u16 v147, v152 offset:648\n"
    "s_waitcnt lgkmcnt(7)\n"
    "v_lshl_or_b32 v200, v140, 16, v200\n"
    "s_waitcnt lgkmcnt(6)\n"
    "v_lshl_or_b32 v201, v141, 16, v201\n"
    "s_waitcnt lgkmcnt(5)\n"
    "v_lshl_or_b32 v202, v142, 16, v202\n"
    "s_waitcnt lgkmcnt(4)\n"
    "v_lshl_or_b32 v203, v143, 16, v203\n"
    "ds_read_u16 v140, v152 offset:32\n"
    "ds_read_u16 v141, v152 offset:240\n"
    "ds_read_u16 v142, v152 offset:448\n"
    "ds_read_u16 v143, v152 offset:656\n"
    "s_waitcnt lgkmcnt(7)\n"
    "v_lshl_or_b32 v208, v144, 16, v208\n"
    "s_add_u32 s75, s74, 3072\n"
    "s_add_u32 s73, s75, 77824\n"
    "s_mov_b32 m0, s73\n"
    "s_nop 0\n"
    "buffer_load_dwordx4 v118, s[44:47], s75 offen lds\n"
    "s_waitcnt lgkmcnt(6)\n"
    "v_lshl_or_b32 v209, v145, 16, v209\n"
    "s_waitcnt lgkmcnt(5)\n"
    "v_lshl_or_b32 v210, v146, 16, v210\n"
    "s_waitcnt lgkmcnt(4)\n"
    "v_lshl_or_b32 v211, v147, 16, v211\n"
    "ds_read_u16 v144, v152 offset:40\n"
    "ds_read_u16 v145, v152 offset:248\n"
    "ds_read_u16 v146, v152 offset:456\n"
    "ds_read_u16 v147, v152 offset:664\n"
    "s_waitcnt lgkmcnt(7)\n"
    "v_lshl_or_b32 v220, v140, 16, v220\n"
    "s_waitcnt lgkmcnt(6)\n"
    "v_lshl_or_b32 v221, v141, 16, v221\n"
    "s_waitcnt lgkmcnt(5)\n"
    "v_lshl_or_b32 v222, v142, 16, v222\n"
    "s_waitcnt lgkmcnt(4)\n"
    "v_lshl_or_b32 v223, v143, 16, v223\n"
    "ds_read_u16 v188, v153\n"
    "ds_read_u16 v190, v153 offset:976\n"
    "ds_read_u16 v196, v153 offset:8\n"
    "ds_read_u16 v198, v153 offset:984\n"
    "ds_read_u16 v204, v153 offset:16\n"
    "s_add_u32 s75, s74, 6144\n"
    "s_add_u32 s73, s75, 75776\n"
    "s_mov_b32 m0, s73\n"
    "s_nop 0\n"
    "buffer_load_dwordx4 v116, s[44:47], s75 offen lds\n"
    "ds_read_u16 v206, v153 offset:992\n"
    "ds_read_u16 v212, v153 offset:24\n"
    "ds_read_u16 v214, v153 offset:1000\n"
    "ds_read_u16 v224, v153 offset:32\n"
    "ds_read_u16 v226, v153 offset:1008\n"
    "ds_read_u16 v236, v153 offset:40\n"
    "ds_read_u16 v238, v153 offset:1016\n"
    "s_waitcnt lgkmcnt(15)\n"
    "v_lshl_or_b32 v232, v144, 16, v232\n"
    "s_waitcnt lgkmcnt(14)\n"
    "v_lshl_or_b32 v233, v145, 16, v233\n"
    "s_waitcnt lgkmcnt(13)\n"
    "v_lshl_or_b32 v234, v146, 16, v234\n"
    "s_waitcnt lgkmcnt(12)\n"
    "v_lshl_or_b32 v235, v147, 16, v235\n"
    "s_waitcnt lgkmcnt(11)\n"
    "v_lshlrev_b32 v188, v154, v188\n"
    "v_and_b32 v189, v159, v188\n"
    "v_and_b32 v188, v155, v188\n"
    "s_waitcnt lgkmcnt(10)\n"
    "v_lshlrev_b32 v190, v154, v190\n"
    "v_and_b32 v191, v159, v190\n"
    "s_add_u32 s75, s74, 6144\n"
    "s_add_u32 s73, s75, 76800\n"
    "s_mov_b32 m0, s73\n"
    "s_nop 0\n"
    "buffer_load_dwordx4 v117, s[44:47], s75 offen lds\n"
    "v_and_b32 v190, v155, v190\n"
    "s_waitcnt lgkmcnt(9)\n"
    "v_lshlrev_b32 v196, v154, v196\n"
    "v_and_b32 v197, v159, v196\n"
    "v_and_b32 v196, v155, v196\n"
    "s_waitcnt lgkmcnt(8)\n"
    "v_lshlrev_b32 v198, v154, v198\n"
    "v_and_b32 v199, v159, v198\n"
    "v_and_b32 v198, v155, v198\n"
    "s_waitcnt lgkmcnt(7)\n"
    "v_lshlrev_b32 v204, v154, v204\n"
    "v_and_b32 v205, v159, v204\n"
    "v_and_b32 v204, v155, v204\n"
    "s_waitcnt lgkmcnt(6)\n"
    "v_lshlrev_b32 v206, v154, v206\n"
    "v_and_b32 v207, v159, v206\n"
    "v_and_b32 v206, v155, v206\n"
    "s_waitcnt lgkmcnt(5)\n"
    "v_lshlrev_b32 v212, v154, v212\n"
    "v_and_b32 v213, v159, v212\n"
    "v_and_b32 v212, v155, v212\n"
    "s_waitcnt lgkmcnt(4)\n"
    "v_lshlrev_b32 v214, v154, v214\n"
    "v_and_b32 v215, v159, v214\n"
    "s_add_u32 s75, s74, 6144\n"
    "s_add_u32 s73, s75, 77824\n"
    "s_mov_b32 m0, s73\n"
    "s_nop 0\n"
    "buffer_load_dwordx4 v118, s[44:47], s75 offen lds\n"
    "v_and_b32 v214, v155, v214\n"
    "s_waitcnt lgkmcnt(3)\n"
    "v_lshlrev_b32 v224, v154, v224\n"
    "v_and_b32 v225, v159, v224\n"
    "v_and_b32 v224, v155, v224\n"
    "s_waitcnt lgkmcnt(2)\n"
    "v_lshlrev_b32 v226, v154, v226\n"
    "v_and_b32 v227, v159, v226\n"
    "v_and_b32 v226, v155, v226\n"
    "s_waitcnt lgkmcnt(1)\n"
    "v_lshlrev_b32 v236, v154, v236\n"
    "v_and_b32 v237, v159, v236\n"
    "v_and_b32 v236, v155, v236\n"
    "s_waitcnt lgkmcnt(0)\n"
    "v_lshlrev_b32 v238, v154, v238\n"
    "v_and_b32 v239, v159, v238\n"
    "v_and_b32 v238, v155, v238\n"
    "s_and_b32 s70, s48, 2\n"
    "s_add_i32 s70, s70, s49\n"
    "s_add_i32 s70, s70, 10\n"
    "s_cmp_lt_i32 s70, 0\n"
    "s_mul_i32 s52, s70, 98304\n"
    "s_cselect_b32 s52, 0x40000000, s52\n"
    "v_and_b32 v144, 31, v0\n"
    "s_add_u32 s75, s74, 9216\n"
    "s_add_u32 s73, s75, 75776\n"
    "s_mov_b32 m0, s73\n"
    "s_nop 0\n"
    "buffer_load_dwordx4 v116, s[44:47], s75 offen lds\n"
    "v_mul_u32_u24 v144, 192, v144\n"
    "v_lshrrev_b32 v145, 5, v0\n"
    "v_lshlrev_b32 v145, 4, v145\n"
    "v_add_u32 v119, v144, v145\n"
    "s_mul_i32 s79, s48, 18432\n"
    "s_add_u32 s78, s79, 6144\n"
    "s_add_u32 s51, s79, 12288\n"
    "s_waitcnt vmcnt(22)\n"
    "ds_write_b64 v246, v[84:85]\n"
    "ds_write_b64 v246, v[86:87] offset:336\n"
    "s_waitcnt vmcnt(21)\n"
    "ds_write_b64 v247, v[88:89]\n"
    "ds_write_b64 v247, v[90:91] offset:336\n"
    "s_add_u32 s75, s74, 9216\n"
    "s_add_u32 s73, s75, 76800\n"
    "s_mov_b32 m0, s73\n"
    "s_nop 0\n"
    "buffer_load_dwordx4 v117, s[44:47], s75 offen lds\n"
    "s_waitcnt vmcnt(21)\n"
    "ds_write_b64 v248, v[92:93]\n"
    "ds_write_b64 v248, v[94:95] offset:336\n"
    "s_waitcnt vmcnt(20)\n"
    "ds_write_b64 v249, v[96:97]\n"
    "ds_write_b64 v249, v[98:99] offset:336\n"
    "s_add_u32 s70, s54, 0x4000\n"
    "s_cmp_ge_u32 s70, 0xc000\n"
    "s_cselect_b32 s53, s76, s77\n"
    "s_add_u32 s54, s54, s53\n"
    "v_add_u32 v246, s53, v246\n"
    "s_add_u32 s75, s74, 9216\n"
    "s_add_u32 s73, s75, 77824\n"
    "s_mov_b32 m0, s73\n"
    "s_nop 0\n"
    "buffer_load_dwordx4 v118, s[44:47], s75 offen lds\n"
    "v_add_u32 v247, s53, v247\n"
    "v_add_u32 v248, s53, v248\n"
    "v_add_u32 v249, s53, v249\n"
    "s_waitcnt vmcnt(20)\n"
    "ds_write_b64 v246, v[100:101]\n"
    "ds_write_b64 v246, v[102:103] offset:336\n"
    "s_waitcnt vmcnt(19)\n"
    "ds_write_b64 v247, v[104:105]\n"
    "ds_write_b64 v247, v[106:107] offset:336\n"
    "s_waitcnt vmcnt(18)\n"
    "ds_write_b64 v248, v[124:125]\n"
    "s_add_u32 s75, s74, 12288\n"
    "s_add_u32 s73, s75, 75776\n"
    "s_mov_b32 m0, s73\n"
    "s_nop 0\n"
    "buffer_load_dwordx4 v116, s[44:47], s75 offen lds\n"
    "ds_write_b64 v248, v[126:127] offset:336\n"
    "s_waitcnt vmcnt(18)\n"
    "ds_write_b64 v249, v[128:129]\n"
    "ds_write_b64 v249, v[130:131] offset:336\n"
    "s_add_u32 s70, s54, 0x4000\n"
    "s_cmp_ge_u32 s70, 0xc000\n"
    "s_cselect_b32 s53, s76, s77\n"
    "s_add_u32 s54, s54, s53\n"
    "v_add_u32 v246, s53, v246\n"
    "v_add_u32 v247, s53, v247\n"
    "v_add_u32 v248, s53, v248\n"
    "v_add_u32 v249, s53, v249\n"
    "s_add_u32 s75, s74, 12288\n"
    "s_add_u32 s73, s75, 76800\n"
    "s_mov_b32 m0, s73\n"
    "s_nop 0\n"
    "buffer_load_dwordx4 v117, s[44:47], s75 offen lds\n"
    "s_waitcnt lgkmcnt(0)\n"
    "s_barrier\n"
    "ds_read_b128 v[124:127], v241 offset:64\n"
    "ds_read_b128 v[140:143], v241\n"
    "ds_read_b128 v[144:147], v241 offset:16\n"
    "ds_read_b128 v[148:151], v241 offset:32\n"
    "ds_read_b128 v[152:155], v241 offset:48\n"
    "ds_read_b128 v[128:131], v241 offset:400\n"
    "s_waitcnt lgkmcnt(5)\n"
    "v_mfma_f32_16x16x32_bf16 v[156:159], v[188:191], v[124:127], 0\n"
    "s_waitcnt lgkmcnt(3)\n"
    "v_smfmac_f32_16x16x64_bf16 v[156:159], a[240:243], v[140:147], v240\n"
    "ds_read_b128 v[140:143], v241 offset:336\n"
    "ds_read_b128 v[144:147], v241 offset:352\n"
    "s_waitcnt lgkmcnt(3)\n"
    "v_smfmac_f32_16x16x64_bf16 v[156:159], v[184:187], v[148:155], v240\n"
    "s_add_u32 s75, s74, 12288\n"
    "s_add_u32 s73, s75, 77824\n"
    "s_mov_b32 m0, s73\n"
    "s_nop 0\n"
    "buffer_load_dwordx4 v118, s[44:47], s75 offen lds\n"
    "ds_read_b128 v[148:151], v241 offset:368\n"
    "ds_read_b128 v[152:155], v241 offset:384\n"
    "ds_read_b128 v[132:135], v241 offset:736\n"
    "v_cvt_pk_bf16_f32 v160, v156, v157\n"
    "v_cvt_pk_bf16_f32 v161, v158, v159\n"
    "s_waitcnt lgkmcnt(5)\n"
    "v_mfma_f32_16x16x32_bf16 v[156:159], v[196:199], v[128:131], 0\n"
    "s_waitcnt lgkmcnt(3)\n"
    "v_smfmac_f32_16x16x64_bf16 v[156:159], a[244:247], v[140:147], v240\n"
    "ds_read_b128 v[140:143], v241 offset:672\n"
    "ds_read_b128 v[144:147], v241 offset:688\n"
    "s_waitcnt lgkmcnt(3)\n"
    "v_smfmac_f32_16x16x64_bf16 v[156:159], v[192:195], v[148:155], v240\n"
    "ds_read_b128 v[148:151], v241 offset:704\n"
    "s_add_u32 s75, s74, 15360\n"
    "s_add_u32 s73, s75, 75776\n"
    "s_mov_b32 m0, s73\n"
    "s_nop 0\n"
    "buffer_load_dwordx4 v116, s[44:47], s75 offen lds\n"
    "ds_read_b128 v[152:155], v241 offset:720\n"
    "ds_read_b128 v[136:139], v241 offset:1072\n"
    "v_cvt_pk_bf16_f32 v162, v156, v157\n"
    "v_cvt_pk_bf16_f32 v163, v158, v159\n"
    "s_waitcnt lgkmcnt(5)\n"
    "v_mfma_f32_16x16x32_bf16 v[156:159], v[204:207], v[132:135], 0\n"
    "s_waitcnt lgkmcnt(3)\n"
    "v_smfmac_f32_16x16x64_bf16 v[156:159], a[248:251], v[140:147], v240\n"
    "ds_read_b128 v[140:143], v241 offset:1008\n"
    "ds_read_b128 v[144:147], v241 offset:1024\n"
    "s_waitcnt lgkmcnt(3)\n"
    "v_smfmac_f32_16x16x64_bf16 v[156:159], v[200:203], v[148:155], v240\n"
    "ds_read_b128 v[148:151], v241 offset:1040\n"
    "ds_read_b128 v[152:155], v241 offset:1056\n"
    "s_add_u32 s75, s74, 15360\n"
    "s_add_u32 s73, s75, 76800\n"
    "s_mov_b32 m0, s73\n"
    "s_nop 0\n"
    "buffer_load_dwordx4 v117, s[44:47], s75 offen lds\n"
    "ds_write_b64 v242, v[160:161]\n"
    "ds_read_b128 v[124:127], v241 offset:1408\n"
    "v_cvt_pk_bf16_f32 v160, v156, v157\n"
    "v_cvt_pk_bf16_f32 v161, v158, v159\n"
    "s_waitcnt lgkmcnt(6)\n"
    "v_mfma_f32_16x16x32_bf16 v[156:159], v[212:215], v[136:139], 0\n"
    "s_waitcnt lgkmcnt(4)\n"
    "v_smfmac_f32_16x16x64_bf16 v[156:159], a[252:255], v[140:147], v240\n"
    "ds_read_b128 v[140:143], v241 offset:1344\n"
    "ds_read_b128 v[144:147], v241 offset:1360\n"
    "s_waitcnt lgkmcnt(4)\n"
    "v_smfmac_f32_16x16x64_bf16 v[156:159], v[208:211], v[148:155], v240\n"
    "ds_read_b128 v[148:151], v241 offset:1376\n"
    "s_add_u32 s75, s74, 15360\n"
    "s_add_u32 s73, s75, 77824\n"
    "s_mov_b32 m0, s73\n"
    "s_nop 0\n"
    "buffer_load_dwordx4 v118, s[44:47], s75 offen lds\n"
    "ds_read_b128 v[152:155], v241 offset:1392\n"
    "ds_write_b64 v242, v[162:163] offset:8\n"
    "ds_read_b128 v[128:131], v241 offset:1744\n"
    "v_cvt_pk_bf16_f32 v162, v156, v157\n"
    "v_cvt_pk_bf16_f32 v163, v158, v159\n"
    "s_waitcnt lgkmcnt(6)\n"
    "v_mfma_f32_16x16x32_bf16 v[156:159], v[224:227], v[124:127], 0\n"
    "s_waitcnt lgkmcnt(4)\n"
    "v_smfmac_f32_16x16x64_bf16 v[156:159], v[216:219], v[140:147], v240\n"
    "ds_read_b128 v[140:143], v241 offset:1680\n"
    "ds_read_b128 v[144:147], v241 offset:1696\n"
    "s_waitcnt lgkmcnt(4)\n"
    "v_smfmac_f32_16x16x64_bf16 v[156:159], v[220:223], v[148:155], v240\n"
    "buffer_load_dwordx4 v[164:167], v250, s[36:39], s52 offen\n"
    "ds_read_b128 v[148:151], v241 offset:1712\n"
    "ds_read_b128 v[152:155], v241 offset:1728\n"
    "ds_write_b64 v242, v[160:161] offset:16\n"
    "v_add_u32 v241, 0x4000, v241\n"
    "v_add_u32 v254, 0xffff4000, v241\n"
    "v_min_u32 v241, v241, v254\n"
    "ds_write_b64 v242, v[162:163] offset:24\n"
    "v_cvt_pk_bf16_f32 v160, v156, v157\n"
    "v_cvt_pk_bf16_f32 v161, v158, v159\n"
    "s_waitcnt lgkmcnt(6)\n"
    "v_mfma_f32_16x16x32_bf16 v[156:159], v[236:239], v[128:131], 0\n"
    "ds_write_b64 v242, v[160:161] offset:32\n"
    "buffer_load_dwordx4 v[168:171], v251, s[36:39], s52 offen\n"
    "s_waitcnt lgkmcnt(5)\n"
    "v_smfmac_f32_16x16x64_bf16 v[156:159], v[228:231], v[140:147], v240\n"
    "s_waitcnt lgkmcnt(3)\n"
    "v_smfmac_f32_16x16x64_bf16 v[156:159], v[232:235], v[148:155], v240\n"
    "s_waitcnt vmcnt(21)\n"
    "s_nop 6\n"
    "v_cvt_pk_bf16_f32 v162, v156, v157\n"
    "v_cvt_pk_bf16_f32 v163, v158, v159\n"
    "ds_write_b64 v242, v[162:163] offset:40\n"
    "ds_write_b64 v246, v[52:53]\n"
    "ds_write_b64 v246, v[54:55] offset:336\n"
    "buffer_load_dwordx4 v[172:175], v252, s[36:39], s52 offen\n"
    "ds_write_b64 v247, v[56:57]\n"
    "ds_write_b64 v247, v[58:59] offset:336\n"
    "ds_write_b64 v248, v[60:61]\n"
    "ds_write_b64 v248, v[62:63] offset:336\n"
    "ds_write_b64 v249, v[64:65]\n"
    "buffer_load_dwordx4 v[176:179], v1, s[36:39], s52 offen\n"
    "ds_write_b64 v249, v[66:67] offset:336\n"
    "s_add_u32 s70, s54, 0x4000\n"
    "s_cmp_ge_u32 s70, 0xc000\n"
    "s_cselect_b32 s53, s76, s77\n"
    "s_add_u32 s54, s54, s53\n"
    "v_add_u32 v246, s53, v246\n"
    "v_add_u32 v247, s53, v247\n"
    "v_add_u32 v248, s53, v248\n"
    "v_add_u32 v249, s53, v249\n"
    "s_waitcnt lgkmcnt(0)\n"
    "s_barrier\n"
    "ds_read_b128 v[124:127], v241 offset:64\n"
    "ds_read_b128 v[140:143], v241\n"
    "ds_read_b128 v[144:147], v241 offset:16\n"
    "ds_read_b128 v[148:151], v241 offset:32\n"
    "ds_read_b128 v[152:155], v241 offset:48\n"
    "buffer_load_dwordx4 v[4:7], v119, s[80:83], s79 offen\n"
    "ds_read_b128 v[128:131], v241 offset:400\n"
    "s_waitcnt lgkmcnt(5)\n"
    "v_mfma_f32_16x16x32_bf16 v[156:159], v[188:191], v[124:127], 0\n"
    "s_waitcnt lgkmcnt(3)\n"
    "v_smfmac_f32_16x16x64_bf16 v[156:159], a[240:243], v[140:147], v240\n"
    "ds_read_b128 v[140:143], v241 offset:336\n"
    "ds_read_b128 v[144:147], v241 offset:352\n"
    "s_waitcnt lgkmcnt(3)\n"
    "v_smfmac_f32_16x16x64_bf16 v[156:159], v[184:187], v[148:155], v240\n"
    "ds_read_b128 v[148:151], v241 offset:368\n"
    "ds_read_b128 v[152:155], v241 offset:384\n"
    "ds_read_b128 v[132:135], v241 offset:736\n"
    "s_nop 4\n"
    "v_cvt_pk_bf16_f32 v160, v156, v157\n"
    "v_cvt_pk_bf16_f32 v161, v158, v159\n"
    "buffer_load_dwordx4 v[8:11], v119, s[80:83], s79 offen offset:32\n"
    "s_waitcnt lgkmcnt(5)\n"
    "v_mfma_f32_16x16x32_bf16 v[156:159], v[196:199], v[128:131], 0\n"
    "s_waitcnt lgkmcnt(3)\n"
    "v_smfmac_f32_16x16x64_bf16 v[156:159], a[244:247], v[140:147], v240\n"
    "ds_read_b128 v[140:143], v241 offset:672\n"
    "ds_read_b128 v[144:147], v241 offset:688\n"
    "s_waitcnt lgkmcnt(3)\n"
    "v_smfmac_f32_16x16x64_bf16 v[156:159], v[192:195], v[148:155], v240\n"
    "ds_read_b128 v[148:151], v241 offset:704\n"
    "ds_read_b128 v[152:155], v241 offset:720\n"
    "ds_read_b128 v[136:139], v241 offset:1072\n"
    "s_nop 4\n"
    "v_cvt_pk_bf16_f32 v162, v156, v157\n"
    "v_cvt_pk_bf16_f32 v163, v158, v159\n"
    "s_waitcnt lgkmcnt(5)\n"
    "v_mfma_f32_16x16x32_bf16 v[156:159], v[204:207], v[132:135], 0\n"
    "buffer_load_dwordx4 v[12:15], v119, s[80:83], s79 offen offset:64\n"
    "s_waitcnt lgkmcnt(3)\n"
    "v_smfmac_f32_16x16x64_bf16 v[156:159], a[248:251], v[140:147], v240\n"
    "ds_read_b128 v[140:143], v241 offset:1008\n"
    "ds_read_b128 v[144:147], v241 offset:1024\n"
    "s_waitcnt lgkmcnt(3)\n"
    "v_smfmac_f32_16x16x64_bf16 v[156:159], v[200:203], v[148:155], v240\n"
    "ds_read_b128 v[148:151], v241 offset:1040\n"
    "ds_read_b128 v[152:155], v241 offset:1056\n"
    "ds_write_b64 v242, v[160:161] offset:13312\n"
    "ds_read_b128 v[124:127], v241 offset:1408\n"
    "s_nop 3\n"
    "v_cvt_pk_bf16_f32 v160, v156, v157\n"
    "v_cvt_pk_bf16_f32 v161, v158, v159\n"
    "buffer_load_dwordx4 v[16:19], v119, s[80:83], s79 offen offset:96\n"
    "s_waitcnt lgkmcnt(6)\n"
    "v_mfma_f32_16x16x32_bf16 v[156:159], v[212:215], v[136:139], 0\n"
    "s_waitcnt lgkmcnt(4)\n"
    "v_smfmac_f32_16x16x64_bf16 v[156:159], a[252:255], v[140:147], v240\n"
    "ds_read_b128 v[140:143], v241 offset:1344\n"
    "ds_read_b128 v[144:147], v241 offset:1360\n"
    "s_waitcnt lgkmcnt(4)\n"
    "v_smfmac_f32_16x16x64_bf16 v[156:159], v[208:211], v[148:155], v240\n"
    "ds_read_b128 v[148:151], v241 offset:1376\n"
    "ds_read_b128 v[152:155], v241 offset:1392\n"
    "ds_write_b64 v242, v[162:163] offset:13320\n"
    "ds_read_b128 v[128:131], v241 offset:1744\n"
    "buffer_load_dwordx4 v[20:23], v119, s[80:83], s79 offen offset:128\n"
    "s_nop 2\n"
    "v_cvt_pk_bf16_f32 v162, v156, v157\n"
    "v_cvt_pk_bf16_f32 v163, v158, v159\n"
    "s_waitcnt lgkmcnt(6)\n"
    "v_mfma_f32_16x16x32_bf16 v[156:159], v[224:227], v[124:127], 0\n"
    "s_waitcnt lgkmcnt(4)\n"
    "v_smfmac_f32_16x16x64_bf16 v[156:159], v[216:219], v[140:147], v240\n"
    "ds_read_b128 v[140:143], v241 offset:1680\n"
    "ds_read_b128 v[144:147], v241 offset:1696\n"
    "s_waitcnt lgkmcnt(4)\n"
    "v_smfmac_f32_16x16x64_bf16 v[156:159], v[220:223], v[148:155], v240\n"
    "ds_read_b128 v[148:151], v241 offset:1712\n"
    "ds_read_b128 v[152:155], v241 offset:1728\n"
    "ds_write_b64 v242, v[160:161] offset:13328\n"
    "buffer_load_dwordx4 v[24:27], v119, s[80:83], s79 offen offset:160\n"
    "v_add_u32 v241, 0x4000, v241\n"
    "v_add_u32 v254, 0xffff4000, v241\n"
    "v_min_u32 v241, v241, v254\n"
    "ds_write_b64 v242, v[162:163] offset:13336\n"
    "v_cvt_pk_bf16_f32 v160, v156, v157\n"
    "v_cvt_pk_bf16_f32 v161, v158, v159\n"
    "s_waitcnt lgkmcnt(6)\n"
    "v_mfma_f32_16x16x32_bf16 v[156:159], v[236:239], v[128:131], 0\n"
    "ds_write_b64 v242, v[160:161] offset:13344\n"
    "s_waitcnt lgkmcnt(5)\n"
    "v_smfmac_f32_16x16x64_bf16 v[156:159], v[228:231], v[140:147], v240\n"
    "s_waitcnt lgkmcnt(3)\n"
    "v_smfmac_f32_16x16x64_bf16 v[156:159], v[232:235], v[148:155], v240\n"
    "buffer_load_dwordx4 v[28:31], v119, s[80:83], s78 offen\n"
    "s_nop 6\n"
    "v_cvt_pk_bf16_f32 v162, v156, v157\n"
    "v_cvt_pk_bf16_f32 v163, v158, v159\n"
    "ds_write_b64 v242, v[162:163] offset:13352\n"
    "s_and_b32 s70, s48, 2\n"
    "s_add_i32 s70, s70, s49\n"
    "s_add_i32 s70, s70, 14\n"
    "s_mul_i32 s52, s70, 98304\n"
    "s_waitcnt lgkmcnt(0)\n"
    "s_waitcnt vmcnt(11)\n"
    "s_nop 0\n"
    "s_barrier\n"
    "ds_read_b128 v[84:87], v243\n"
    "ds_read_b128 v[88:91], v243 offset:32\n"
    "ds_read_b128 v[92:95], v243 offset:64\n"
    "ds_read_b128 v[96:99], v243 offset:96\n"
    "ds_read_b128 v[100:103], v243 offset:128\n"
    "ds_read_b128 v[104:107], v243 offset:160\n"
    "buffer_load_dwordx4 v[32:35], v119, s[80:83], s78 offen offset:32\n"
    "v_lshrrev_b32 v120, 5, v0\n"
    "v_lshrrev_b32 v121, 4, v0\n"
    "v_and_b32 v121, 1, v121\n"
    "v_lshrrev_b32 v122, 2, v0\n"
    "v_and_b32 v122, 3, v122\n"
    "v_and_b32 v123, 3, v0\n"
    "v_lshlrev_b32 v120, 3, v120\n"
    "v_add_u32 v120, v120, v122\n"
    "v_mul_u32_u24 v120, 0x300, v120\n"
    "v_lshlrev_b32 v121, 5, v121\n"
    "v_lshlrev_b32 v123, 3, v123\n"
    "v_add3_u32 v120, v120, v121, v123\n"
    "v_add_u32 v120, 75776, v120\n"
    "v_xor_b32 v121, 0, v122\n"
    "v_lshlrev_b32 v121, 6, v121\n"
    "v_add_u32 v160, v120, v121\n"
    "v_xor_b32 v121, 1, v122\n"
    "v_lshlrev_b32 v121, 6, v121\n"
    "buffer_load_dwordx4 v[36:39], v119, s[80:83], s78 offen offset:64\n"
    "v_add_u32 v161, v120, v121\n"
    "v_xor_b32 v121, 2, v122\n"
    "v_lshlrev_b32 v121, 6, v121\n"
    "v_add_u32 v162, v120, v121\n"
    "v_xor_b32 v121, 3, v122\n"
    "v_lshlrev_b32 v121, 6, v121\n"
    "v_add_u32 v163, v120, v121\n"
    "v_mov_b32 v156, 0\n"
    "v_mov_b32 v157, 0\n"
    "v_mov_b32 v158, 0\n"
    "v_mov_b32 v159, 0\n"
    "s_waitcnt lgkmcnt(5)\n"
    "v_dot2c_f32_bf16 v156, v84, v84\n"
    "v_dot2c_f32_bf16 v158, 0x3f803f80, v84\n"
    "v_dot2c_f32_bf16 v157, v85, v85\n"
    "v_dot2c_f32_bf16 v159, 0x3f803f80, v85\n"
    "v_dot2c_f32_bf16 v156, v86, v86\n"
    "v_dot2c_f32_bf16 v158, 0x3f803f80, v86\n"
    "v_dot2c_f32_bf16 v157, v87, v87\n"
    "buffer_load_dwordx4 v[40:43], v119, s[80:83], s78 offen offset:96\n"
    "v_dot2c_f32_bf16 v159, 0x3f803f80, v87\n"
    "s_waitcnt lgkmcnt(4)\n"
    "v_dot2c_f32_bf16 v156, v88, v88\n"
    "v_dot2c_f32_bf16 v158, 0x3f803f80, v88\n"
    "v_dot2c_f32_bf16 v157, v89, v89\n"
    "v_dot2c_f32_bf16 v159, 0x3f803f80, v89\n"
    "v_dot2c_f32_bf16 v156, v90, v90\n"
    "v_dot2c_f32_bf16 v158, 0x3f803f80, v90\n"
    "v_dot2c_f32_bf16 v157, v91, v91\n"
    "v_dot2c_f32_bf16 v159, 0x3f803f80, v91\n"
    "s_waitcnt lgkmcnt(3)\n"
    "v_dot2c_f32_bf16 v156, v92, v92\n"
    "v_dot2c_f32_bf16 v158, 0x3f803f80, v92\n"
    "v_dot2c_f32_bf16 v157, v93, v93\n"
    "v_dot2c_f32_bf16 v159, 0x3f803f80, v93\n"
    "v_dot2c_f32_bf16 v156, v94, v94\n"
    "v_dot2c_f32_bf16 v158, 0x3f803f80, v94\n"
    "v_dot2c_f32_bf16 v157, v95, v95\n"
    "v_dot2c_f32_bf16 v159, 0x3f803f80, v95\n"
    "s_waitcnt lgkmcnt(2)\n"
    "v_dot2c_f32_bf16 v156, v96, v96\n"
    "buffer_load_dwordx4 v[44:47], v119, s[80:83], s78 offen offset:128\n"
    "v_dot2c_f32_bf16 v158, 0x3f803f80, v96\n"
    "v_dot2c_f32_bf16 v157, v97, v97\n"
    "v_dot2c_f32_bf16 v159, 0x3f803f80, v97\n"
    "v_dot2c_f32_bf16 v156, v98, v98\n"
    "v_dot2c_f32_bf16 v158, 0x3f803f80, v98\n"
    "v_dot2c_f32_bf16 v157, v99, v99\n"
    "v_dot2c_f32_bf16 v159, 0x3f803f80, v99\n"
    "s_waitcnt lgkmcnt(1)\n"
    "v_dot2c_f32_bf16 v156, v100, v100\n"
    "v_dot2c_f32_bf16 v158, 0x3f803f80, v100\n"
    "v_dot2c_f32_bf16 v157, v101, v101\n"
    "v_dot2c_f32_bf16 v159, 0x3f803f80, v101\n"
    "v_dot2c_f32_bf16 v156, v102, v102\n"
    "v_dot2c_f32_bf16 v158, 0x3f803f80, v102\n"
    "v_dot2c_f32_bf16 v157, v103, v103\n"
    "v_dot2c_f32_bf16 v159, 0x3f803f80, v103\n"
    "s_waitcnt lgkmcnt(0)\n"
    "v_dot2c_f32_bf16 v156, v104, v104\n"
    "v_dot2c_f32_bf16 v158, 0x3f803f80, v104\n"
    "v_dot2c_f32_bf16 v157, v105, v105\n"
    "buffer_load_dwordx4 v[48:51], v119, s[80:83], s78 offen offset:160\n"
    "v_dot2c_f32_bf16 v159, 0x3f803f80, v105\n"
    "v_dot2c_f32_bf16 v156, v106, v106\n"
    "v_dot2c_f32_bf16 v158, 0x3f803f80, v106\n"
    "v_dot2c_f32_bf16 v157, v107, v107\n"
    "v_dot2c_f32_bf16 v159, 0x3f803f80, v107\n"
    "s_waitcnt vmcnt(15)\n"
    "ds_write_b64 v246, v[164:165]\n"
    "ds_write_b64 v246, v[166:167] offset:336\n"
    "s_waitcnt vmcnt(14)\n"
    "ds_write_b64 v247, v[168:169]\n"
    "ds_write_b64 v247, v[170:171] offset:336\n"
    "buffer_load_dwordx4 v[68:71], v119, s[80:83], s51 offen\n"
    "s_waitcnt vmcnt(14)\n"
    "ds_write_b64 v248, v[172:173]\n"
    "ds_write_b64 v248, v[174:175] offset:336\n"
    "s_waitcnt vmcnt(13)\n"
    "ds_write_b64 v249, v[176:177]\n"
    "ds_write_b64 v249, v[178:179] offset:336\n"
    "s_add_u32 s70, s54, 0x4000\n"
    "s_cmp_ge_u32 s70, 0xc000\n"
    "s_cselect_b32 s53, s76, s77\n"
    "s_add_u32 s54, s54, s53\n"
    "v_add_u32 v246, s53, v246\n"
    "buffer_load_dwordx4 v[72:75], v119, s[80:83], s51 offen offset:32\n"
    "v_add_u32 v247, s53, v247\n"
    "v_add_u32 v248, s53, v248\n"
    "v_add_u32 v249, s53, v249\n"
    "buffer_load_dwordx4 v[76:79], v119, s[80:83], s51 offen offset:64\n"
    "buffer_load_dwordx4 v[80:83], v119, s[80:83], s51 offen offset:96\n"
    "buffer_load_dwordx4 v[108:111], v119, s[80:83], s51 offen offset:128\n"
    "buffer_load_dwordx4 v[112:115], v119, s[80:83], s51 offen offset:160\n"
    "s_mov_b32 s79, 0x3c2aaaab\n"
    "s_cmp_lt_u32 s48, 2\n"
    "s_cselect_b32 s72, 512, 0\n"
    "s_and_b32 s71, s48, 1\n"
    "s_mul_i32 s70, s71, 36864\n"
    "s_add_u32 s72, s72, s70\n"
    "s_cmp_lt_u32 s48, 2\n"
    "s_cselect_b64 vcc, -1, 0\n"
    "v_cndmask_b32 v176, v160, v163, vcc\n"
    "v_add_u32 v176, s72, v176\n"
    "ds_read_b64_tr_b16 v[124:125], v160\n"
    "ds_read_b64_tr_b16 v[126:127], v160 offset:3072\n"
    "ds_read_b64_tr_b16 v[128:129], v160 offset:12288\n"
    "ds_read_b64_tr_b16 v[130:131], v160 offset:15360\n"
    "ds_read_b64_tr_b16 v[132:133], v160 offset:24576\n"
    "ds_read_b64_tr_b16 v[134:135], v160 offset:27648\n"
    "ds_read_b64_tr_b16 v[136:137], v160 offset:36864\n"
    "ds_read_b64_tr_b16 v[138:139], v160 offset:39936\n"
    "ds_read_b64_tr_b16 v[164:165], v176\n"
    "ds_read_b64_tr_b16 v[166:167], v176 offset:3072\n"
    "ds_read_b64_tr_b16 v[168:169], v176 offset:12288\n"
    "ds_read_b64_tr_b16 v[170:171], v176 offset:15360\n"
    "ds_read_b64_tr_b16 v[172:173], v176 offset:24576\n"
    "ds_read_b64_tr_b16 v[174:175], v176 offset:27648\n"
    "ds_read_b64_tr_b16 v[148:149], v160 offset:49152\n"
    "ds_read_b64_tr_b16 v[150:151], v160 offset:52224\n"
    "ds_read_b64_tr_b16 v[152:153], v160 offset:61440\n"
    "ds_read_b64_tr_b16 v[154:155], v160 offset:64512\n"
    "v_add_f32 v156, v156, v157\n"
    "v_add_f32 v158, v158, v159\n"
    "v_mov_b32 v157, v156\n"
    "v_mov_b32 v159, v158\n"
    "s_nop 0\n"
    "v_permlane32_swap_b32 v156, v157\n"
    "v_permlane32_swap_b32 v158, v159\n"
    "v_add_f32 v158, v158, v159\n"
    "v_add_f32 v156, v156, v157\n"
    "v_mul_f32 v254, 0x3c2aaaab, v158\n"
    "v_mul_f32 v255, v254, v254\n"
    "v_fma_f32 v255, v156, s79, -v255\n"
    "v_max_f32 v255, 0, v255\n"
    "v_add_f32 v255, 0x3727c5ac, v255\n"
    "v_rsq_f32 v255, v255\n"
    "s_nop 0\n"
    "v_mul_f32 v255, 0xbeeff558, v255\n"
    "v_mul_f32 v254, v254, v255\n"
    "s_waitcnt lgkmcnt(0)\n"
    "ds_read_b64_tr_b16 a[0:1], v161\n"
    "ds_read_b64_tr_b16 a[2:3], v161 offset:3072\n"
    "ds_read_b64_tr_b16 a[4:5], v161 offset:12288\n"
    "ds_read_b64_tr_b16 a[6:7], v161 offset:15360\n"
    "ds_read_b64_tr_b16 a[8:9], v161 offset:24576\n"
    "ds_read_b64_tr_b16 a[10:11], v161 offset:27648\n"
    "ds_read_b64_tr_b16 a[12:13], v161 offset:36864\n"
    "ds_read_b64_tr_b16 a[14:15], v161 offset:39936\n"
    "ds_read_b64_tr_b16 a[16:17], v161 offset:49152\n"
    "ds_read_b64_tr_b16 a[18:19], v161 offset:52224\n"
    "ds_read_b64_tr_b16 a[20:21], v161 offset:61440\n"
    "ds_read_b64_tr_b16 a[22:23], v161 offset:64512\n"
    "ds_read_b64_tr_b16 a[24:25], v162\n"
    "ds_read_b64_tr_b16 a[26:27], v162 offset:3072\n"
    "ds_read_b64_tr_b16 a[28:29], v162 offset:12288\n"
    "ds_read_b64_tr_b16 a[30:31], v162 offset:15360\n"
    "ds_read_b64_tr_b16 a[32:33], v162 offset:24576\n"
    "ds_read_b64_tr_b16 a[34:35], v162 offset:27648\n"
    "ds_read_b64_tr_b16 a[36:37], v162 offset:36864\n"
    "ds_read_b64_tr_b16 a[38:39], v162 offset:39936\n"
    "v_lshlrev_b32 v120, 16, v84\n"
    "v_and_b32 v121, s59, v84\n"
    "v_fma_f32 v120, v120, v255, -v254\n"
    "v_fma_f32 v121, v121, v255, -v254\n"
    "v_cvt_pk_bf16_f32 v84, v120, v121\n"
    "v_lshlrev_b32 v122, 16, v85\n"
    "v_and_b32 v123, s59, v85\n"
    "v_fma_f32 v122, v122, v255, -v254\n"
    "v_fma_f32 v123, v123, v255, -v254\n"
    "v_cvt_pk_bf16_f32 v85, v122, v123\n"
    "v_lshlrev_b32 v120, 16, v86\n"
    "v_and_b32 v121, s59, v86\n"
    "v_fma_f32 v120, v120, v255, -v254\n"
    "v_fma_f32 v121, v121, v255, -v254\n"
    "v_cvt_pk_bf16_f32 v86, v120, v121\n"
    "v_lshlrev_b32 v122, 16, v87\n"
    "v_and_b32 v123, s59, v87\n"
    "v_fma_f32 v122, v122, v255, -v254\n"
    "v_fma_f32 v123, v123, v255, -v254\n"
    "v_cvt_pk_bf16_f32 v87, v122, v123\n"
    "s_nop 1\n"
    "v_mfma_f32_32x32x16_bf16 v[52:67], v[124:127], v[84:87], 0\n"
    "ds_read_b64_tr_b16 a[40:41], v162 offset:49152\n"
    "ds_read_b64_tr_b16 a[42:43], v162 offset:52224\n"
    "ds_read_b64_tr_b16 a[44:45], v162 offset:61440\n"
    "ds_read_b64_tr_b16 a[46:47], v162 offset:64512\n"
    "ds_read_b64_tr_b16 a[48:49], v163\n"
    "ds_read_b64_tr_b16 a[50:51], v163 offset:3072\n"
    "ds_read_b64_tr_b16 a[52:53], v163 offset:12288\n"
    "ds_read_b64_tr_b16 a[54:55], v163 offset:15360\n"
    "ds_read_b64_tr_b16 a[56:57], v163 offset:24576\n"
    "ds_read_b64_tr_b16 a[58:59], v163 offset:27648\n"
    "ds_read_b64_tr_b16 a[60:61], v163 offset:36864\n"
    "ds_read_b64_tr_b16 a[62:63], v163 offset:39936\n"
    "ds_read_b64_tr_b16 a[64:65], v163 offset:49152\n"
    "ds_read_b64_tr_b16 a[66:67], v163 offset:52224\n"
    "ds_read_b64_tr_b16 a[68:69], v163 offset:61440\n"
    "ds_read_b64_tr_b16 a[70:71], v163 offset:64512\n"
    "ds_read_b64_tr_b16 a[72:73], v160 offset:256\n"
    "ds_read_b64_tr_b16 a[74:75], v160 offset:3328\n"
    "ds_read_b64_tr_b16 a[76:77], v160 offset:12544\n"
    "ds_read_b64_tr_b16 a[78:79], v160 offset:15616\n"
    "v_lshlrev_b32 v120, 16, v88\n"
    "v_and_b32 v121, s59, v88\n"
    "v_fma_f32 v120, v120, v255, -v254\n"
    "v_fma_f32 v121, v121, v255, -v254\n"
    "v_cvt_pk_bf16_f32 v88, v120, v121\n"
    "v_lshlrev_b32 v122, 16, v89\n"
    "v_and_b32 v123, s59, v89\n"
    "v_fma_f32 v122, v122, v255, -v254\n"
    "v_fma_f32 v123, v123, v255, -v254\n"
    "v_cvt_pk_bf16_f32 v89, v122, v123\n"
    "v_lshlrev_b32 v120, 16, v90\n"
    "v_and_b32 v121, s59, v90\n"
    "v_fma_f32 v120, v120, v255, -v254\n"
    "v_fma_f32 v121, v121, v255, -v254\n"
    "v_cvt_pk_bf16_f32 v90, v120, v121\n"
    "v_lshlrev_b32 v122, 16, v91\n"
    "v_and_b32 v123, s59, v91\n"
    "v_fma_f32 v122, v122, v255, -v254\n"
    "v_fma_f32 v123, v123, v255, -v254\n"
    "v_cvt_pk_bf16_f32 v91, v122, v123\n"
    "s_nop 1\n"
    "v_mfma_f32_32x32x16_bf16 v[52:67], v[128:131], v[88:91], v[52:67]\n"
    "ds_read_b64_tr_b16 a[80:81], v160 offset:24832\n"
    "ds_read_b64_tr_b16 a[82:83], v160 offset:27904\n"
    "ds_read_b64_tr_b16 a[84:85], v160 offset:37120\n"
    "ds_read_b64_tr_b16 a[86:87], v160 offset:40192\n"
    "ds_read_b64_tr_b16 a[88:89], v160 offset:49408\n"
    "ds_read_b64_tr_b16 a[90:91], v160 offset:52480\n"
    "ds_read_b64_tr_b16 a[92:93], v160 offset:61696\n"
    "ds_read_b64_tr_b16 a[94:95], v160 offset:64768\n"
    "ds_read_b64_tr_b16 a[96:97], v161 offset:256\n"
    "ds_read_b64_tr_b16 a[98:99], v161 offset:3328\n"
    "ds_read_b64_tr_b16 a[100:101], v161 offset:12544\n"
    "ds_read_b64_tr_b16 a[102:103], v161 offset:15616\n"
    "ds_read_b64_tr_b16 a[104:105], v161 offset:24832\n"
    "ds_read_b64_tr_b16 a[106:107], v161 offset:27904\n"
    "ds_read_b64_tr_b16 a[108:109], v161 offset:37120\n"
    "ds_read_b64_tr_b16 a[110:111], v161 offset:40192\n"
    "ds_read_b64_tr_b16 a[112:113], v161 offset:49408\n"
    "ds_read_b64_tr_b16 a[114:115], v161 offset:52480\n"
    "ds_read_b64_tr_b16 a[116:117], v161 offset:61696\n"
    "ds_read_b64_tr_b16 a[118:119], v161 offset:64768\n"
    "v_lshlrev_b32 v120, 16, v92\n"
    "v_and_b32 v121, s59, v92\n"
    "v_fma_f32 v120, v120, v255, -v254\n"
    "v_fma_f32 v121, v121, v255, -v254\n"
    "v_cvt_pk_bf16_f32 v92, v120, v121\n"
    "v_lshlrev_b32 v122, 16, v93\n"
    "v_and_b32 v123, s59, v93\n"
    "v_fma_f32 v122, v122, v255, -v254\n"
    "v_fma_f32 v123, v123, v255, -v254\n"
    "v_cvt_pk_bf16_f32 v93, v122, v123\n"
    "v_lshlrev_b32 v120, 16, v94\n"
    "v_and_b32 v121, s59, v94\n"
    "v_fma_f32 v120, v120, v255, -v254\n"
    "v_fma_f32 v121, v121, v255, -v254\n"
    "v_cvt_pk_bf16_f32 v94, v120, v121\n"
    "v_lshlrev_b32 v122, 16, v95\n"
    "v_and_b32 v123, s59, v95\n"
    "v_fma_f32 v122, v122, v255, -v254\n"
    "v_fma_f32 v123, v123, v255, -v254\n"
    "v_cvt_pk_bf16_f32 v95, v122, v123\n"
    "s_nop 1\n"
    "v_mfma_f32_32x32x16_bf16 v[52:67], v[132:135], v[92:95], v[52:67]\n"
    "ds_read_b64_tr_b16 a[120:121], v162 offset:256\n"
    "ds_read_b64_tr_b16 a[122:123], v162 offset:3328\n"
    "ds_read_b64_tr_b16 a[124:125], v162 offset:12544\n"
    "ds_read_b64_tr_b16 a[126:127], v162 offset:15616\n"
    "ds_read_b64_tr_b16 a[128:129], v162 offset:24832\n"
    "ds_read_b64_tr_b16 a[130:131], v162 offset:27904\n"
    "ds_read_b64_tr_b16 a[132:133], v162 offset:37120\n"
    "ds_read_b64_tr_b16 a[134:135], v162 offset:40192\n"
    "ds_read_b64_tr_b16 a[136:137], v162 offset:49408\n"
    "ds_read_b64_tr_b16 a[138:139], v162 offset:52480\n"
    "ds_read_b64_tr_b16 a[140:141], v162 offset:61696\n"
    "ds_read_b64_tr_b16 a[142:143], v162 offset:64768\n"
    "ds_read_b64_tr_b16 a[144:145], v163 offset:256\n"
    "ds_read_b64_tr_b16 a[146:147], v163 offset:3328\n"
    "ds_read_b64_tr_b16 a[148:149], v163 offset:12544\n"
    "ds_read_b64_tr_b16 a[150:151], v163 offset:15616\n"
    "ds_read_b64_tr_b16 a[152:153], v163 offset:24832\n"
    "ds_read_b64_tr_b16 a[154:155], v163 offset:27904\n"
    "ds_read_b64_tr_b16 a[156:157], v163 offset:37120\n"
    "ds_read_b64_tr_b16 a[158:159], v163 offset:40192\n"
    "v_lshlrev_b32 v120, 16, v96\n"
    "v_and_b32 v121, s59, v96\n"
    "v_fma_f32 v120, v120, v255, -v254\n"
    "v_fma_f32 v121, v121, v255, -v254\n"
    "v_cvt_pk_bf16_f32 v96, v120, v121\n"
    "v_lshlrev_b32 v122, 16, v97\n"
    "v_and_b32 v123, s59, v97\n"
    "v_fma_f32 v122, v122, v255, -v254\n"
    "v_fma_f32 v123, v123, v255, -v254\n"
    "v_cvt_pk_bf16_f32 v97, v122, v123\n"
    "v_lshlrev_b32 v120, 16, v98\n"
    "v_and_b32 v121, s59, v98\n"
    "v_fma_f32 v120, v120, v255, -v254\n"
    "v_fma_f32 v121, v121, v255, -v254\n"
    "v_cvt_pk_bf16_f32 v98, v120, v121\n"
    "v_lshlrev_b32 v122, 16, v99\n"
    "v_and_b32 v123, s59, v99\n"
    "v_fma_f32 v122, v122, v255, -v254\n"
    "v_fma_f32 v123, v123, v255, -v254\n"
    "v_cvt_pk_bf16_f32 v99, v122, v123\n"
    "s_nop 1\n"
    "v_mfma_f32_32x32x16_bf16 v[52:67], v[136:139], v[96:99], v[52:67]\n"
    "ds_read_b64_tr_b16 a[160:161], v163 offset:49408\n"
    "ds_read_b64_tr_b16 a[162:163], v163 offset:52480\n"
    "ds_read_b64_tr_b16 a[164:165], v163 offset:61696\n"
    "ds_read_b64_tr_b16 a[166:167], v163 offset:64768\n"
    "ds_read_b64_tr_b16 a[168:169], v160 offset:512\n"
    "ds_read_b64_tr_b16 a[170:171], v160 offset:3584\n"
    "ds_read_b64_tr_b16 a[172:173], v160 offset:12800\n"
    "ds_read_b64_tr_b16 a[174:175], v160 offset:15872\n"
    "ds_read_b64_tr_b16 a[176:177], v160 offset:25088\n"
    "ds_read_b64_tr_b16 a[178:179], v160 offset:28160\n"
    "ds_read_b64_tr_b16 a[180:181], v160 offset:37376\n"
    "ds_read_b64_tr_b16 a[182:183], v160 offset:40448\n"
    "ds_read_b64_tr_b16 a[184:185], v160 offset:49664\n"
    "ds_read_b64_tr_b16 a[186:187], v160 offset:52736\n"
    "ds_read_b64_tr_b16 a[188:189], v160 offset:61952\n"
    "ds_read_b64_tr_b16 a[190:191], v160 offset:65024\n"
    "ds_read_b64_tr_b16 a[192:193], v161 offset:512\n"
    "ds_read_b64_tr_b16 a[194:195], v161 offset:3584\n"
    "ds_read_b64_tr_b16 a[196:197], v161 offset:12800\n"
    "ds_read_b64_tr_b16 a[198:199], v161 offset:15872\n"
    "v_lshlrev_b32 v120, 16, v100\n"
    "v_and_b32 v121, s59, v100\n"
    "v_fma_f32 v120, v120, v255, -v254\n"
    "v_fma_f32 v121, v121, v255, -v254\n"
    "v_cvt_pk_bf16_f32 v100, v120, v121\n"
    "v_lshlrev_b32 v122, 16, v101\n"
    "v_and_b32 v123, s59, v101\n"
    "v_fma_f32 v122, v122, v255, -v254\n"
    "v_fma_f32 v123, v123, v255, -v254\n"
    "v_cvt_pk_bf16_f32 v101, v122, v123\n"
    "v_lshlrev_b32 v120, 16, v102\n"
    "v_and_b32 v121, s59, v102\n"
    "v_fma_f32 v120, v120, v255, -v254\n"
    "v_fma_f32 v121, v121, v255, -v254\n"
    "v_cvt_pk_bf16_f32 v102, v120, v121\n"
    "v_lshlrev_b32 v122, 16, v103\n"
    "v_and_b32 v123, s59, v103\n"
    "v_fma_f32 v122, v122, v255, -v254\n"
    "v_fma_f32 v123, v123, v255, -v254\n"
    "v_cvt_pk_bf16_f32 v103, v122, v123\n"
    "s_nop 1\n"
    "v_mfma_f32_32x32x16_bf16 v[52:67], v[148:151], v[100:103], v[52:67]\n"
    "ds_read_b64_tr_b16 a[200:201], v161 offset:25088\n"
    "ds_read_b64_tr_b16 a[202:203], v161 offset:28160\n"
    "ds_read_b64_tr_b16 a[204:205], v161 offset:37376\n"
    "ds_read_b64_tr_b16 a[206:207], v161 offset:40448\n"
    "ds_read_b64_tr_b16 a[208:209], v161 offset:49664\n"
    "ds_read_b64_tr_b16 a[210:211], v161 offset:52736\n"
    "ds_read_b64_tr_b16 a[212:213], v161 offset:61952\n"
    "ds_read_b64_tr_b16 a[214:215], v161 offset:65024\n"
    "ds_read_b64_tr_b16 a[216:217], v162 offset:512\n"
    "ds_read_b64_tr_b16 a[218:219], v162 offset:3584\n"
    "ds_read_b64_tr_b16 a[220:221], v162 offset:12800\n"
    "ds_read_b64_tr_b16 a[222:223], v162 offset:15872\n"
    "ds_read_b64_tr_b16 a[224:225], v162 offset:25088\n"
    "ds_read_b64_tr_b16 a[226:227], v162 offset:28160\n"
    "ds_read_b64_tr_b16 a[228:229], v162 offset:37376\n"
    "ds_read_b64_tr_b16 a[230:231], v162 offset:40448\n"
    "ds_read_b64_tr_b16 a[232:233], v162 offset:49664\n"
    "ds_read_b64_tr_b16 a[234:235], v162 offset:52736\n"
    "ds_read_b64_tr_b16 a[236:237], v162 offset:61952\n"
    "ds_read_b64_tr_b16 a[238:239], v162 offset:65024\n"
    "v_lshlrev_b32 v120, 16, v104\n"
    "v_and_b32 v121, s59, v104\n"
    "v_fma_f32 v120, v120, v255, -v254\n"
    "v_fma_f32 v121, v121, v255, -v254\n"
    "v_cvt_pk_bf16_f32 v104, v120, v121\n"
    "v_lshlrev_b32 v122, 16, v105\n"
    "v_and_b32 v123, s59, v105\n"
    "v_fma_f32 v122, v122, v255, -v254\n"
    "v_fma_f32 v123, v123, v255, -v254\n"
    "v_cvt_pk_bf16_f32 v105, v122, v123\n"
    "v_lshlrev_b32 v120, 16, v106\n"
    "v_and_b32 v121, s59, v106\n"
    "v_fma_f32 v120, v120, v255, -v254\n"
    "v_fma_f32 v121, v121, v255, -v254\n"
    "v_cvt_pk_bf16_f32 v106, v120, v121\n"
    "v_lshlrev_b32 v122, 16, v107\n"
    "v_and_b32 v123, s59, v107\n"
    "v_fma_f32 v122, v122, v255, -v254\n"
    "v_fma_f32 v123, v123, v255, -v254\n"
    "v_cvt_pk_bf16_f32 v107, v122, v123\n"
    "s_nop 1\n"
    "v_mfma_f32_32x32x16_bf16 v[52:67], v[152:155], v[104:107], v[52:67]\n"
    "s_waitcnt vmcnt(17)\n"
    "v_and_b32 v120, 31, v0\n"
    "v_lshrrev_b32 v121, 5, v0\n"
    "v_lshrrev_b32 v122, 2, v120\n"
    "v_and_b32 v122, 1, v122\n"
    "v_xor_b32 v122, v122, v121\n"
    "v_and_b32 v121, 3, v120\n"
    "v_lshrrev_b32 v123, 3, v120\n"
    "v_and_b32 v123, 1, v123\n"
    "v_lshl_add_u32 v121, v123, 2, v121\n"
    "v_and_b32 v123, 1, v121\n"
    "v_lshlrev_b32 v123, 4, v123\n"
    "v_mov_b32 v254, 0x3f80\n"
    "v_lshlrev_b32 v254, v123, v254\n"
    "v_lshrrev_b32 v121, 1, v121\n"
    "v_lshrrev_b32 v120, 4, v120\n"
    "v_lshl_add_u32 v121, v120, 2, v121\n"
    "v_lshl_add_u32 v121, v122, 3, v121\n"
    "v_cmp_eq_u32 vcc, 0, v121\n"
    "s_nop 1\n"
    "v_cndmask_b32 v116, 0, v254, vcc\n"
    "v_cmp_eq_u32 vcc, 1, v121\n"
    "s_nop 1\n"
    "v_cndmask_b32 v117, 0, v254, vcc\n"
    "v_cmp_eq_u32 vcc, 2, v121\n"
    "s_nop 1\n"
    "v_cndmask_b32 v118, 0, v254, vcc\n"
    "v_cmp_eq_u32 vcc, 3, v121\n"
    "s_nop 1\n"
    "v_cndmask_b32 v119, 0, v254, vcc\n"
    "v_cmp_eq_u32 vcc, 4, v121\n"
    "s_nop 1\n"
    "v_cndmask_b32 v180, 0, v254, vcc\n"
    "v_cmp_eq_u32 vcc, 5, v121\n"
    "s_nop 1\n"
    "v_cndmask_b32 v181, 0, v254, vcc\n"
    "v_cmp_eq_u32 vcc, 6, v121\n"
    "s_nop 1\n"
    "v_cndmask_b32 v182, 0, v254, vcc\n"
    "v_cmp_eq_u32 vcc, 7, v121\n"
    "s_nop 1\n"
    "v_cndmask_b32 v183, 0, v254, vcc\n"
    "s_mul_i32 s70, s48, 3072\n"
    "s_add_u32 s70, s70, 73728\n"
    "v_add_u32 v176, s70, v244\n"
    "s_mul_i32 s70, s48, 18432\n"
    "v_add_u32 v177, s70, v244\n"
    "v_mfma_f32_32x32x16_bf16 v[140:155], v[4:7], v[116:119], 0\n"
    "s_waitcnt vmcnt(16)\n"
    "v_mfma_f32_32x32x16_bf16 v[140:155], v[8:11], v[180:183], v[140:155]\n"
    "s_waitcnt vmcnt(15)\n"
    "v_mfma_f32_32x32x16_bf16 v[124:139], v[12:15], v[116:119], 0\n"
    "s_waitcnt vmcnt(14)\n"
    "v_mfma_f32_32x32x16_bf16 v[124:139], v[16:19], v[180:183], v[124:139]\n"
    "ds_write_b128 v176, v[164:167]\n"
    "ds_write_b128 v176, v[168:171] offset:1024\n"
    "ds_write_b128 v176, v[172:175] offset:2048\n"
    "ds_write_b128 v245, v[116:119] offset:20480\n"
    "ds_write_b128 v245, v[180:183] offset:21504\n"
    "s_waitcnt lgkmcnt(0)\n"
    "s_barrier\n"
    "s_nop 0\n"
    "v_cvt_pk_f16_f32 v4, v140, v141\n"
    "v_cvt_pk_f16_f32 v5, v142, v143\n"
    "v_cvt_pk_f16_f32 v6, v144, v145\n"
    "v_cvt_pk_f16_f32 v7, v146, v147\n"
    "ds_write_b128 v177, v[4:7]\n"
    "v_cvt_pk_f16_f32 v8, v148, v149\n"
    "v_cvt_pk_f16_f32 v9, v150, v151\n"
    "v_cvt_pk_f16_f32 v10, v152, v153\n"
    "v_cvt_pk_f16_f32 v11, v154, v155\n"
    "ds_write_b128 v177, v[8:11] offset:3072\n"
    "s_waitcnt vmcnt(13)\n"
    "v_mfma_f32_32x32x16_bf16 v[140:155], v[20:23], v[116:119], 0\n"
    "s_waitcnt vmcnt(12)\n"
    "v_mfma_f32_32x32x16_bf16 v[140:155], v[24:27], v[180:183], v[140:155]\n"
    "s_waitcnt vmcnt(11)\n"
    "v_cvt_pk_f16_f32 v12, v124, v125\n"
    "v_cvt_pk_f16_f32 v13, v126, v127\n"
    "v_cvt_pk_f16_f32 v14, v128, v129\n"
    "v_cvt_pk_f16_f32 v15, v130, v131\n"
    "ds_write_b128 v177, v[12:15] offset:1024\n"
    "v_cvt_pk_f16_f32 v16, v132, v133\n"
    "v_cvt_pk_f16_f32 v17, v134, v135\n"
    "v_cvt_pk_f16_f32 v18, v136, v137\n"
    "v_cvt_pk_f16_f32 v19, v138, v139\n"
    "ds_write_b128 v177, v[16:19] offset:4096\n"
    "v_mfma_f32_32x32x16_bf16 v[124:139], v[28:31], v[116:119], 0\n"
    "s_waitcnt vmcnt(10)\n"
    "v_mfma_f32_32x32x16_bf16 v[124:139], v[32:35], v[180:183], v[124:139]\n"
    "s_waitcnt vmcnt(9)\n"
    "v_cvt_pk_f16_f32 v20, v140, v141\n"
    "v_cvt_pk_f16_f32 v21, v142, v143\n"
    "v_cvt_pk_f16_f32 v22, v144, v145\n"
    "v_cvt_pk_f16_f32 v23, v146, v147\n"
    "ds_write_b128 v177, v[20:23] offset:2048\n"
    "v_cvt_pk_f16_f32 v24, v148, v149\n"
    "v_cvt_pk_f16_f32 v25, v150, v151\n"
    "v_cvt_pk_f16_f32 v26, v152, v153\n"
    "v_cvt_pk_f16_f32 v27, v154, v155\n"
    "ds_write_b128 v177, v[24:27] offset:5120\n"
    "v_mfma_f32_32x32x16_bf16 v[140:155], v[36:39], v[116:119], 0\n"
    "s_waitcnt vmcnt(8)\n"
    "v_mfma_f32_32x32x16_bf16 v[140:155], v[40:43], v[180:183], v[140:155]\n"
    "s_waitcnt vmcnt(7)\n"
    "v_cvt_pk_f16_f32 v28, v124, v125\n"
    "v_cvt_pk_f16_f32 v29, v126, v127\n"
    "v_cvt_pk_f16_f32 v30, v128, v129\n"
    "v_cvt_pk_f16_f32 v31, v130, v131\n"
    "ds_write_b128 v177, v[28:31] offset:6144\n"
    "v_cvt_pk_f16_f32 v32, v132, v133\n"
    "v_cvt_pk_f16_f32 v33, v134, v135\n"
    "v_cvt_pk_f16_f32 v34, v136, v137\n"
    "v_cvt_pk_f16_f32 v35, v138, v139\n"
    "ds_write_b128 v177, v[32:35] offset:9216\n"
    "v_mfma_f32_32x32x16_bf16 v[124:139], v[44:47], v[116:119], 0\n"
    "s_waitcnt vmcnt(6)\n"
    "v_mfma_f32_32x32x16_bf16 v[124:139], v[48:51], v[180:183], v[124:139]\n"
    "s_waitcnt vmcnt(5)\n"
    "v_cvt_pk_f16_f32 v36, v140, v141\n"
    "v_cvt_pk_f16_f32 v37, v142, v143\n"
    "v_cvt_pk_f16_f32 v38, v144, v145\n"
    "v_cvt_pk_f16_f32 v39, v146, v147\n"
    "ds_write_b128 v177, v[36:39] offset:7168\n"
    "v_cvt_pk_f16_f32 v40, v148, v149\n"
    "v_cvt_pk_f16_f32 v41, v150, v151\n"
    "v_cvt_pk_f16_f32 v42, v152, v153\n"
    "v_cvt_pk_f16_f32 v43, v154, v155\n"
    "ds_write_b128 v177, v[40:43] offset:10240\n"
    "v_mfma_f32_32x32x16_bf16 v[140:155], v[68:71], v[116:119], 0\n"
    "s_waitcnt vmcnt(4)\n"
    "v_mfma_f32_32x32x16_bf16 v[140:155], v[72:75], v[180:183], v[140:155]\n"
    "s_waitcnt vmcnt(3)\n"
    "v_cvt_pk_f16_f32 v44, v124, v125\n"
    "v_cvt_pk_f16_f32 v45, v126, v127\n"
    "v_cvt_pk_f16_f32 v46, v128, v129\n"
    "v_cvt_pk_f16_f32 v47, v130, v131\n"
    "ds_write_b128 v177, v[44:47] offset:8192\n"
    "v_cvt_pk_f16_f32 v48, v132, v133\n"
    "v_cvt_pk_f16_f32 v49, v134, v135\n"
    "v_cvt_pk_f16_f32 v50, v136, v137\n"
    "v_cvt_pk_f16_f32 v51, v138, v139\n"
    "ds_write_b128 v177, v[48:51] offset:11264\n"
    "v_mfma_f32_32x32x16_bf16 v[124:139], v[76:79], v[116:119], 0\n"
    "s_waitcnt vmcnt(2)\n"
    "v_mfma_f32_32x32x16_bf16 v[124:139], v[80:83], v[180:183], v[124:139]\n"
    "s_waitcnt vmcnt(1)\n"
    "v_cvt_pk_f16_f32 v68, v140, v141\n"
    "v_cvt_pk_f16_f32 v69, v142, v143\n"
    "v_cvt_pk_f16_f32 v70, v144, v145\n"
    "v_cvt_pk_f16_f32 v71, v146, v147\n"
    "ds_write_b128 v177, v[68:71] offset:12288\n"
    "v_cvt_pk_f16_f32 v72, v148, v149\n"
    "v_cvt_pk_f16_f32 v73, v150, v151\n"
    "v_cvt_pk_f16_f32 v74, v152, v153\n"
    "v_cvt_pk_f16_f32 v75, v154, v155\n"
    "ds_write_b128 v177, v[72:75] offset:15360\n"
    "v_mfma_f32_32x32x16_bf16 v[140:155], v[108:111], v[116:119], 0\n"
    "s_waitcnt vmcnt(0)\n"
    "v_mfma_f32_32x32x16_bf16 v[140:155], v[112:115], v[180:183], v[140:155]\n"
    "v_cvt_pk_f16_f32 v76, v124, v125\n"
    "v_cvt_pk_f16_f32 v77, v126, v127\n"
    "v_cvt_pk_f16_f32 v78, v128, v129\n"
    "v_cvt_pk_f16_f32 v79, v130, v131\n"
    "ds_write_b128 v177, v[76:79] offset:13312\n"
    "v_cvt_pk_f16_f32 v80, v132, v133\n"
    "v_cvt_pk_f16_f32 v81, v134, v135\n"
    "v_cvt_pk_f16_f32 v82, v136, v137\n"
    "v_cvt_pk_f16_f32 v83, v138, v139\n"
    "ds_write_b128 v177, v[80:83] offset:16384\n"
    "s_nop 1\n"
    "v_cvt_pk_f16_f32 v108, v140, v141\n"
    "v_cvt_pk_f16_f32 v109, v142, v143\n"
    "v_cvt_pk_f16_f32 v110, v144, v145\n"
    "v_cvt_pk_f16_f32 v111, v146, v147\n"
    "ds_write_b128 v177, v[108:111] offset:14336\n"
    "v_cvt_pk_f16_f32 v112, v148, v149\n"
    "v_cvt_pk_f16_f32 v113, v150, v151\n"
    "v_cvt_pk_f16_f32 v114, v152, v153\n"
    "v_cvt_pk_f16_f32 v115, v154, v155\n"
    "ds_write_b128 v177, v[112:115] offset:17408\n"
    "s_mov_b64 s[40:41], %5\n"
    "s_and_b32 s41, s41, 0xffff\n"
    "s_mov_b32 s42, 50331648\n"
    "s_mov_b32 s43, 0x20000\n"
    "s_waitcnt lgkmcnt(0)\n"
    "s_barrier\n"
    "ds_read_b128 v[124:127], v244\n"
    "ds_read_b128 v[128:131], v244 offset:1024\n"
    "ds_read_b128 v[132:135], v244 offset:2048\n"
    "s_mov_b32 s51, 7\n"
    "s_mov_b32 s78, 0x40000000\n"
    "s_waitcnt vmcnt(0) lgkmcnt(0)\n"
    ".p2align 5\n"
    "L_loop_%=:\n"
    "v_cvt_pk_bf16_f32 v20, v20, v21\n"
    "v_cvt_pk_bf16_f32 v21, v22, v23\n"
    "v_cvt_pk_bf16_f32 v22, v24, v25\n"
    "v_cvt_pk_bf16_f32 v23, v26, v27\n"
    "v_cvt_pk_bf16_f32 v28, v28, v29\n"
    "v_cvt_pk_bf16_f32 v29, v30, v31\n"
    "v_cvt_pk_f16_f32 v108, v52, v53\n"
    "v_cvt_pk_bf16_f32 v30, v32, v33\n"
    "v_mfma_f32_32x32x16_bf16 v[68:83], a[0:3], v[84:87], 0\n"
    "ds_read_b128 v[136:139], v241 offset:64\n"
    "v_cvt_pk_f16_f32 v109, v54, v55\n"
    "v_cvt_pk_bf16_f32 v31, v34, v35\n"
    "v_permlane16_swap_b32 v20, v28\n"
    "v_cvt_pk_f16_f32 v110, v56, v57\n"
    "v_cvt_pk_f16_f32 v111, v58, v59\n"
    "v_permlane16_swap_b32 v21, v29\n"
    "v_pk_fma_f16 v116, v108, v108, s57\n"
    "v_pk_fma_f16 v117, v109, v109, s57\n"
    "v_permlane16_swap_b32 v22, v30\n"
    "v_pk_fma_f16 v118, v110, v110, s57\n"
    "v_pk_fma_f16 v119, v111, v111, s57\n"
    "v_permlane16_swap_b32 v23, v31\n"
    "v_pk_mul_f16 v116, v116, v108\n"
    "v_pk_mul_f16 v117, v117, v109\n"
    "v_pk_mul_f16 v118, v118, v110\n"
    "buffer_store_dwordx4 v[20:23], v2, s[40:43], s55 offen offset:64 nt\n"
    "v_pk_mul_f16 v119, v119, v111\n"
    "v_exp_f16_e32 v120, v116\n"
    "v_exp_f16_e32 v121, v117\n"
    "v_mfma_f32_32x32x16_bf16 v[68:83], a[4:7], v[88:91], v[68:83]\n"
    "ds_read_b128 v[140:143], v241\n"
    "ds_read_b128 v[144:147], v241 offset:16\n"
    "v_exp_f16_e32 v122, v118\n"
    "v_exp_f16_e32 v123, v119\n"
    "v_exp_f16_sdwa v120, v116 dst_sel:WORD_1 dst_unused:UNUSED_PRESERVE src0_sel:WORD_1\n"
    "v_exp_f16_sdwa v121, v117 dst_sel:WORD_1 dst_unused:UNUSED_PRESERVE src0_sel:WORD_1\n"
    "buffer_store_dwordx4 v[28:31], v2, s[40:43], s78 offen offset:64 nt\n"
    "v_exp_f16_sdwa v122, v118 dst_sel:WORD_1 dst_unused:UNUSED_PRESERVE src0_sel:WORD_1\n"
    "v_exp_f16_sdwa v123, v119 dst_sel:WORD_1 dst_unused:UNUSED_PRESERVE src0_sel:WORD_1\n"
    "v_pk_fma_f16 v116, v120, s58, s58\n"
    "v_pk_fma_f16 v117, v121, s58, s58\n"
    "v_mfma_f32_32x32x16_bf16 v[68:83], a[8:11], v[92:95], v[68:83]\n"
    "s_waitcnt lgkmcnt(2)\n"
    "ds_read_b128 v[148:151], v241 offset:32\n"
    "ds_read_b128 v[152:155], v241 offset:48\n"
    "v_pk_fma_f16 v118, v122, s58, s58\n"
    "v_pk_fma_f16 v119, v123, s58, s58\n"
    "v_rcp_f16_e32 v120, v116\n"
    "v_rcp_f16_e32 v121, v117\n"
    "v_rcp_f16_e32 v122, v118\n"
    "v_mfma_f32_16x16x32_bf16 v[156:159], v[188:191], v[136:139], 0\n"
    "v_rcp_f16_e32 v123, v119\n"
    "v_rcp_f16_sdwa v120, v116 dst_sel:WORD_1 dst_unused:UNUSED_PRESERVE src0_sel:WORD_1\n"
    "v_cvt_pk_bf16_f32 v36, v36, v37\n"
    "v_cvt_pk_bf16_f32 v37, v38, v39\n"
    "v_rcp_f16_sdwa v121, v117 dst_sel:WORD_1 dst_unused:UNUSED_PRESERVE src0_sel:WORD_1\n"
    "v_cvt_pk_bf16_f32 v38, v40, v41\n"
    "v_mfma_f32_32x32x16_bf16 v[68:83], a[12:15], v[96:99], v[68:83]\n"
    "s_waitcnt lgkmcnt(2)\n"
    "v_rcp_f16_sdwa v122, v118 dst_sel:WORD_1 dst_unused:UNUSED_PRESERVE src0_sel:WORD_1\n"
    "v_cvt_pk_bf16_f32 v39, v42, v43\n"
    "v_cvt_pk_bf16_f32 v44, v44, v45\n"
    "v_rcp_f16_sdwa v123, v119 dst_sel:WORD_1 dst_unused:UNUSED_PRESERVE src0_sel:WORD_1\n"
    "v_cvt_pk_bf16_f32 v45, v46, v47\n"
    "v_pk_mul_f16 v108, v108, v120\n"
    "v_cvt_pk_bf16_f32 v46, v48, v49\n"
    "v_pk_mul_f16 v109, v109, v121\n"
    "v_cvt_pk_bf16_f32 v47, v50, v51\n"
    "v_pk_mul_f16 v110, v110, v122\n"
    "v_permlane16_swap_b32 v36, v44\n"
    "v_smfmac_f32_16x16x64_bf16 v[156:159], a[240:243], v[140:147], v240\n"
    "v_pk_mul_f16 v111, v111, v123\n"
    "v_cvt_pk_f16_f32 v112, v60, v61\n"
    "v_cvt_pk_f16_f32 v113, v62, v63\n"
    "v_permlane16_swap_b32 v37, v45\n"
    "v_cvt_pk_f16_f32 v114, v64, v65\n"
    "v_cvt_pk_f16_f32 v115, v66, v67\n"
    "v_permlane16_swap_b32 v38, v46\n"
    "v_pk_fma_f16 v116, v112, v112, s57\n"
    "v_pk_fma_f16 v117, v113, v113, s57\n"
    "v_pk_fma_f16 v118, v114, v114, s57\n"
    "v_mfma_f32_32x32x16_bf16 v[68:83], a[16:19], v[100:103], v[68:83]\n"
    "v_permlane16_swap_b32 v39, v47\n"
    "v_pk_fma_f16 v119, v115, v115, s57\n"
    "v_pk_mul_f16 v116, v116, v112\n"
    "v_pk_mul_f16 v117, v117, v113\n"
    "buffer_store_dwordx4 v[36:39], v2, s[40:43], s55 offen offset:128 nt\n"
    "v_pk_mul_f16 v118, v118, v114\n"
    "v_pk_mul_f16 v119, v119, v115\n"
    "v_exp_f16_e32 v120, v116\n"
    "v_mfma_f32_32x32x16_f16 v[4:19], v[124:127], v[108:111], 0\n"
    "ds_read_b128 v[124:127], v244 offset:3072\n"
    "v_exp_f16_e32 v121, v117\n"
    "v_exp_f16_e32 v122, v118\n"
    "v_exp_f16_e32 v123, v119\n"
    "v_exp_f16_sdwa v120, v116 dst_sel:WORD_1 dst_unused:UNUSED_PRESERVE src0_sel:WORD_1\n"
    "v_mfma_f32_32x32x16_bf16 v[68:83], a[20:23], v[104:107], v[68:83]\n"
    "s_waitcnt lgkmcnt(1)\n"
    "v_exp_f16_sdwa v121, v117 dst_sel:WORD_1 dst_unused:UNUSED_PRESERVE src0_sel:WORD_1\n"
    "v_exp_f16_sdwa v122, v118 dst_sel:WORD_1 dst_unused:UNUSED_PRESERVE src0_sel:WORD_1\n"
    "buffer_store_dwordx4 v[44:47], v2, s[40:43], s78 offen offset:128 nt\n"
    "v_exp_f16_sdwa v123, v119 dst_sel:WORD_1 dst_unused:UNUSED_PRESERVE src0_sel:WORD_1\n"
    "v_smfmac_f32_16x16x64_bf16 v[156:159], v[184:187], v[148:155], v240\n"
    "v_pk_fma_f16 v116, v120, s58, s58\n"
    "v_pk_fma_f16 v117, v121, s58, s58\n"
    "v_pk_fma_f16 v118, v122, s58, s58\n"
    "v_pk_fma_f16 v119, v123, s58, s58\n"
    "v_rcp_f16_e32 v120, v116\n"
    "v_rcp_f16_e32 v121, v117\n"
    "v_mfma_f32_32x32x16_f16 v[20:35], v[128:131], v[108:111], 0\n"
    "ds_read_b128 v[128:131], v244 offset:4096\n"
    "v_cvt_pk_bf16_f32 v160, v156, v157\n"
    "v_cvt_pk_bf16_f32 v161, v158, v159\n"
    "v_rcp_f16_e32 v122, v118\n"
    "v_rcp_f16_e32 v123, v119\n"
    "v_rcp_f16_sdwa v120, v116 dst_sel:WORD_1 dst_unused:UNUSED_PRESERVE src0_sel:WORD_1\n"
    "v_rcp_f16_sdwa v121, v117 dst_sel:WORD_1 dst_unused:UNUSED_PRESERVE src0_sel:WORD_1\n"
    "v_rcp_f16_sdwa v122, v118 dst_sel:WORD_1 dst_unused:UNUSED_PRESERVE src0_sel:WORD_1\n"
    "v_rcp_f16_sdwa v123, v119 dst_sel:WORD_1 dst_unused:UNUSED_PRESERVE src0_sel:WORD_1\n"
    "v_pk_mul_f16 v112, v112, v120\n"
    "s_barrier\n"
    "v_mfma_f32_32x32x16_f16 v[36:51], v[132:135], v[108:111], 0\n"
    "s_waitcnt lgkmcnt(1)\n"
    "ds_read_b128 v[132:135], v244 offset:5120\n"
    "ds_write_b64 v242, v[160:161]\n"
    "v_pk_mul_f16 v113, v113, v121\n"
    "v_pk_mul_f16 v114, v114, v122\n"
    "v_pk_mul_f16 v115, v115, v123\n"
    "v_cvt_pk_f16_f32 v108, v68, v69\n"
    "v_cvt_pk_f16_f32 v109, v70, v71\n"
    "v_mfma_f32_32x32x16_f16 v[4:19], v[124:127], v[112:115], v[4:19]\n"
    "ds_read_b128 v[136:139], v241 offset:400\n"
    "v_cvt_pk_f16_f32 v110, v72, v73\n"
    "v_cvt_pk_f16_f32 v111, v74, v75\n"
    "v_pk_fma_f16 v116, v108, v108, s57\n"
    "v_pk_fma_f16 v117, v109, v109, s57\n"
    "v_pk_fma_f16 v118, v110, v110, s57\n"
    "v_mfma_f32_32x32x16_bf16 v[52:67], a[24:27], v[84:87], 0\n"
    "s_waitcnt lgkmcnt(3)\n"
    "ds_read_b128 v[140:143], v241 offset:336\n"
    "v_pk_fma_f16 v119, v111, v111, s57\n"
    "v_pk_mul_f16 v116, v116, v108\n"
    "v_pk_mul_f16 v117, v117, v109\n"
    "v_pk_mul_f16 v118, v118, v110\n"
    "v_pk_mul_f16 v119, v119, v111\n"
    "v_exp_f16_e32 v120, v116\n"
    "v_mfma_f32_32x32x16_f16 v[20:35], v[128:131], v[112:115], v[20:35]\n"
    "ds_read_b128 v[144:147], v241 offset:352\n"
    "v_exp_f16_e32 v121, v117\n"
    "v_exp_f16_e32 v122, v118\n"
    "v_exp_f16_e32 v123, v119\n"
    "v_exp_f16_sdwa v120, v116 dst_sel:WORD_1 dst_unused:UNUSED_PRESERVE src0_sel:WORD_1\n"
    "v_mfma_f32_32x32x16_bf16 v[52:67], a[28:31], v[88:91], v[52:67]\n"
    "s_waitcnt lgkmcnt(4)\n"
    "ds_read_b128 v[148:151], v241 offset:368\n"
    "v_exp_f16_sdwa v121, v117 dst_sel:WORD_1 dst_unused:UNUSED_PRESERVE src0_sel:WORD_1\n"
    "v_exp_f16_sdwa v122, v118 dst_sel:WORD_1 dst_unused:UNUSED_PRESERVE src0_sel:WORD_1\n"
    "v_exp_f16_sdwa v123, v119 dst_sel:WORD_1 dst_unused:UNUSED_PRESERVE src0_sel:WORD_1\n"
    "v_mfma_f32_32x32x16_f16 v[36:51], v[132:135], v[112:115], v[36:51]\n"
    "ds_read_b128 v[152:155], v241 offset:384\n"
    "v_pk_fma_f16 v116, v120, s58, s58\n"
    "v_pk_fma_f16 v117, v121, s58, s58\n"
    "v_pk_fma_f16 v118, v122, s58, s58\n"
    "v_pk_fma_f16 v119, v123, s58, s58\n"
    "v_rcp_f16_e32 v120, v116\n"
    "v_mfma_f32_32x32x16_bf16 v[52:67], a[32:35], v[92:95], v[52:67]\n"
    "ds_read_b128 v[124:127], v244 offset:6144\n"
    "v_rcp_f16_e32 v121, v117\n"
    "v_rcp_f16_e32 v122, v118\n"
    "v_rcp_f16_e32 v123, v119\n"
    "v_rcp_f16_sdwa v120, v116 dst_sel:WORD_1 dst_unused:UNUSED_PRESERVE src0_sel:WORD_1\n"
    "v_mfma_f32_32x32x16_bf16 v[52:67], a[36:39], v[96:99], v[52:67]\n"
    "s_waitcnt lgkmcnt(5)\n"
    "ds_read_b128 v[128:131], v244 offset:7168\n"
    "v_rcp_f16_sdwa v121, v117 dst_sel:WORD_1 dst_unused:UNUSED_PRESERVE src0_sel:WORD_1\n"
    "v_rcp_f16_sdwa v122, v118 dst_sel:WORD_1 dst_unused:UNUSED_PRESERVE src0_sel:WORD_1\n"
    "v_rcp_f16_sdwa v123, v119 dst_sel:WORD_1 dst_unused:UNUSED_PRESERVE src0_sel:WORD_1\n"
    "v_mfma_f32_16x16x32_bf16 v[156:159], v[196:199], v[136:139], 0\n"
    "ds_read_b128 v[132:135], v244 offset:8192\n"
    "v_pk_mul_f16 v108, v108, v120\n"
    "v_pk_mul_f16 v109, v109, v121\n"
    "v_pk_mul_f16 v110, v110, v122\n"
    "v_pk_mul_f16 v111, v111, v123\n"
    "v_cvt_pk_f16_f32 v112, v76, v77\n"
    "v_cvt_pk_f16_f32 v113, v78, v79\n"
    "v_cvt_pk_f16_f32 v114, v80, v81\n"
    "v_mfma_f32_32x32x16_bf16 v[52:67], a[40:43], v[100:103], v[52:67]\n"
    "s_waitcnt lgkmcnt(5)\n"
    "buffer_load_dwordx4 v[164:167], v250, s[36:39], s52 offen\n"
    "v_cvt_pk_f16_f32 v115, v82, v83\n"
    "v_pk_fma_f16 v116, v112, v112, s57\n"
    "v_pk_fma_f16 v117, v113, v113, s57\n"
    "v_pk_fma_f16 v118, v114, v114, s57\n"
    "v_pk_fma_f16 v119, v115, v115, s57\n"
    "v_pk_mul_f16 v116, v116, v112\n"
    "v_pk_mul_f16 v117, v117, v113\n"
    "v_smfmac_f32_16x16x64_bf16 v[156:159], a[244:247], v[140:147], v240\n"
    "buffer_load_dwordx4 v[168:171], v251, s[36:39], s52 offen\n"
    "v_pk_mul_f16 v118, v118, v114\n"
    "v_pk_mul_f16 v119, v119, v115\n"
    "v_exp_f16_e32 v120, v116\n"
    "v_exp_f16_e32 v121, v117\n"
    "v_mfma_f32_32x32x16_bf16 v[52:67], a[44:47], v[104:107], v[52:67]\n"
    "s_waitcnt lgkmcnt(2)\n"
    "buffer_load_dwordx4 v[172:175], v252, s[36:39], s52 offen\n"
    "v_exp_f16_e32 v122, v118\n"
    "v_exp_f16_e32 v123, v119\n"
    "v_exp_f16_sdwa v120, v116 dst_sel:WORD_1 dst_unused:UNUSED_PRESERVE src0_sel:WORD_1\n"
    "v_mfma_f32_32x32x16_f16 v[4:19], v[124:127], v[108:111], v[4:19]\n"
    "ds_read_b128 v[124:127], v244 offset:9216\n"
    "v_exp_f16_sdwa v121, v117 dst_sel:WORD_1 dst_unused:UNUSED_PRESERVE src0_sel:WORD_1\n"
    "v_exp_f16_sdwa v122, v118 dst_sel:WORD_1 dst_unused:UNUSED_PRESERVE src0_sel:WORD_1\n"
    "v_exp_f16_sdwa v123, v119 dst_sel:WORD_1 dst_unused:UNUSED_PRESERVE src0_sel:WORD_1\n"
    "v_smfmac_f32_16x16x64_bf16 v[156:159], v[192:195], v[148:155], v240\n"
    "s_waitcnt lgkmcnt(2)\n"
    "buffer_load_dwordx4 v[176:179], v1, s[36:39], s52 offen\n"
    "s_add_u32 s52, s52, 393216\n"
    "v_pk_fma_f16 v116, v120, s58, s58\n"
    "v_pk_fma_f16 v117, v121, s58, s58\n"
    "v_pk_fma_f16 v118, v122, s58, s58\n"
    "v_pk_fma_f16 v119, v123, s58, s58\n"
    "v_rcp_f16_e32 v120, v116\n"
    "v_rcp_f16_e32 v121, v117\n"
    "v_mfma_f32_32x32x16_f16 v[20:35], v[128:131], v[108:111], v[20:35]\n"
    "s_waitcnt lgkmcnt(1)\n"
    "ds_read_b128 v[128:131], v244 offset:10240\n"
    "v_cvt_pk_bf16_f32 v162, v156, v157\n"
    "v_cvt_pk_bf16_f32 v163, v158, v159\n"
    "v_rcp_f16_e32 v122, v118\n"
    "v_rcp_f16_e32 v123, v119\n"
    "v_rcp_f16_sdwa v120, v116 dst_sel:WORD_1 dst_unused:UNUSED_PRESERVE src0_sel:WORD_1\n"
    "v_rcp_f16_sdwa v121, v117 dst_sel:WORD_1 dst_unused:UNUSED_PRESERVE src0_sel:WORD_1\n"
    "v_rcp_f16_sdwa v122, v118 dst_sel:WORD_1 dst_unused:UNUSED_PRESERVE src0_sel:WORD_1\n"
    "v_rcp_f16_sdwa v123, v119 dst_sel:WORD_1 dst_unused:UNUSED_PRESERVE src0_sel:WORD_1\n"
    "v_mfma_f32_32x32x16_f16 v[36:51], v[132:135], v[108:111], v[36:51]\n"
    "s_waitcnt lgkmcnt(1)\n"
    "ds_read_b128 v[132:135], v244 offset:11264\n"
    "ds_write_b64 v242, v[162:163] offset:8\n"
    "v_pk_mul_f16 v112, v112, v120\n"
    "v_pk_mul_f16 v113, v113, v121\n"
    "v_pk_mul_f16 v114, v114, v122\n"
    "v_pk_mul_f16 v115, v115, v123\n"
    "v_cvt_pk_f16_f32 v108, v52, v53\n"
    "v_cvt_pk_f16_f32 v109, v54, v55\n"
    "v_mfma_f32_32x32x16_f16 v[4:19], v[124:127], v[112:115], v[4:19]\n"
    "ds_read_b128 v[136:139], v241 offset:736\n"
    "v_cvt_pk_f16_f32 v110, v56, v57\n"
    "v_cvt_pk_f16_f32 v111, v58, v59\n"
    "v_pk_fma_f16 v116, v108, v108, s57\n"
    "v_pk_fma_f16 v117, v109, v109, s57\n"
    "v_mfma_f32_32x32x16_bf16 v[68:83], a[48:51], v[84:87], 0\n"
    "s_waitcnt lgkmcnt(3)\n"
    "ds_read_b128 v[140:143], v241 offset:672\n"
    "v_pk_fma_f16 v118, v110, v110, s57\n"
    "v_pk_fma_f16 v119, v111, v111, s57\n"
    "v_pk_mul_f16 v116, v116, v108\n"
    "v_pk_mul_f16 v117, v117, v109\n"
    "v_pk_mul_f16 v118, v118, v110\n"
    "v_pk_mul_f16 v119, v119, v111\n"
    "v_mfma_f32_32x32x16_f16 v[20:35], v[128:131], v[112:115], v[20:35]\n"
    "ds_read_b128 v[144:147], v241 offset:688\n"
    "v_exp_f16_e32 v120, v116\n"
    "v_exp_f16_e32 v121, v117\n"
    "v_exp_f16_e32 v122, v118\n"
    "v_mfma_f32_32x32x16_bf16 v[68:83], a[52:55], v[88:91], v[68:83]\n"
    "s_waitcnt lgkmcnt(4)\n"
    "ds_read_b128 v[148:151], v241 offset:704\n"
    "v_exp_f16_e32 v123, v119\n"
    "v_exp_f16_sdwa v120, v116 dst_sel:WORD_1 dst_unused:UNUSED_PRESERVE src0_sel:WORD_1\n"
    "v_exp_f16_sdwa v121, v117 dst_sel:WORD_1 dst_unused:UNUSED_PRESERVE src0_sel:WORD_1\n"
    "v_mfma_f32_32x32x16_f16 v[36:51], v[132:135], v[112:115], v[36:51]\n"
    "ds_read_b128 v[152:155], v241 offset:720\n"
    "v_exp_f16_sdwa v122, v118 dst_sel:WORD_1 dst_unused:UNUSED_PRESERVE src0_sel:WORD_1\n"
    "v_exp_f16_sdwa v123, v119 dst_sel:WORD_1 dst_unused:UNUSED_PRESERVE src0_sel:WORD_1\n"
    "v_pk_fma_f16 v116, v120, s58, s58\n"
    "v_mfma_f32_32x32x16_bf16 v[68:83], a[56:59], v[92:95], v[68:83]\n"
    "s_waitcnt lgkmcnt(4)\n"
    "v_pk_fma_f16 v117, v121, s58, s58\n"
    "v_pk_fma_f16 v118, v122, s58, s58\n"
    "v_pk_fma_f16 v119, v123, s58, s58\n"
    "v_rcp_f16_e32 v120, v116\n"
    "v_rcp_f16_e32 v121, v117\n"
    "v_mfma_f32_16x16x32_bf16 v[156:159], v[204:207], v[136:139], 0\n"
    "s_waitcnt lgkmcnt(2)\n"
    "ds_read_b128 v[124:127], v244 offset:12288\n"
    "v_rcp_f16_e32 v122, v118\n"
    "v_rcp_f16_e32 v123, v119\n"
    "v_rcp_f16_sdwa v120, v116 dst_sel:WORD_1 dst_unused:UNUSED_PRESERVE src0_sel:WORD_1\n"
    "v_smfmac_f32_16x16x64_bf16 v[156:159], a[248:251], v[140:147], v240\n"
    "ds_read_b128 v[136:139], v241 offset:1072\n"
    "ds_read_b128 v[140:143], v241 offset:1008\n"
    "v_rcp_f16_sdwa v121, v117 dst_sel:WORD_1 dst_unused:UNUSED_PRESERVE src0_sel:WORD_1\n"
    "v_rcp_f16_sdwa v122, v118 dst_sel:WORD_1 dst_unused:UNUSED_PRESERVE src0_sel:WORD_1\n"
    "v_mfma_f32_32x32x16_bf16 v[68:83], a[60:63], v[96:99], v[68:83]\n"
    "s_waitcnt lgkmcnt(3)\n"
    "ds_read_b128 v[128:131], v244 offset:13312\n"
    "v_rcp_f16_sdwa v123, v119 dst_sel:WORD_1 dst_unused:UNUSED_PRESERVE src0_sel:WORD_1\n"
    "v_pk_mul_f16 v108, v108, v120\n"
    "v_pk_mul_f16 v109, v109, v121\n"
    "v_pk_mul_f16 v110, v110, v122\n"
    "v_pk_mul_f16 v111, v111, v123\n"
    "v_smfmac_f32_16x16x64_bf16 v[156:159], v[200:203], v[148:155], v240\n"
    "ds_read_b128 v[132:135], v244 offset:14336\n"
    "ds_read_b128 v[144:147], v241 offset:1024\n"
    "v_cvt_pk_f16_f32 v112, v60, v61\n"
    "v_cvt_pk_f16_f32 v113, v62, v63\n"
    "v_cvt_pk_f16_f32 v114, v64, v65\n"
    "v_cvt_pk_f16_f32 v115, v66, v67\n"
    "v_pk_fma_f16 v116, v112, v112, s57\n"
    "v_pk_fma_f16 v117, v113, v113, s57\n"
    "ds_read_b128 v[148:151], v241 offset:1040\n"
    "v_pk_fma_f16 v118, v114, v114, s57\n"
    "v_cvt_pk_bf16_f32 v160, v156, v157\n"
    "v_cvt_pk_bf16_f32 v161, v158, v159\n"
    "v_pk_fma_f16 v119, v115, v115, s57\n"
    "v_pk_mul_f16 v116, v116, v112\n"
    "v_pk_mul_f16 v117, v117, v113\n"
    "v_pk_mul_f16 v118, v118, v114\n"
    "v_pk_mul_f16 v119, v119, v115\n"
    "v_mfma_f32_32x32x16_bf16 v[68:83], a[64:67], v[100:103], v[68:83]\n"
    "s_waitcnt lgkmcnt(5)\n"
    "ds_write_b64 v242, v[160:161] offset:16\n"
    "ds_read_b128 v[152:155], v241 offset:1056\n"
    "v_exp_f16_e32 v120, v116\n"
    "v_exp_f16_e32 v121, v117\n"
    "v_exp_f16_e32 v122, v118\n"
    "v_mfma_f32_16x16x32_bf16 v[156:159], v[212:215], v[136:139], 0\n"
    "v_exp_f16_e32 v123, v119\n"
    "v_exp_f16_sdwa v120, v116 dst_sel:WORD_1 dst_unused:UNUSED_PRESERVE src0_sel:WORD_1\n"
    "v_exp_f16_sdwa v121, v117 dst_sel:WORD_1 dst_unused:UNUSED_PRESERVE src0_sel:WORD_1\n"
    "v_mfma_f32_32x32x16_bf16 v[68:83], a[68:71], v[104:107], v[68:83]\n"
    "v_exp_f16_sdwa v122, v118 dst_sel:WORD_1 dst_unused:UNUSED_PRESERVE src0_sel:WORD_1\n"
    "v_exp_f16_sdwa v123, v119 dst_sel:WORD_1 dst_unused:UNUSED_PRESERVE src0_sel:WORD_1\n"
    "v_pk_fma_f16 v116, v120, s58, s58\n"
    "v_mfma_f32_32x32x16_f16 v[4:19], v[124:127], v[108:111], v[4:19]\n"
    "s_waitcnt lgkmcnt(3)\n"
    "ds_read_b128 v[124:127], v244 offset:15360\n"
    "v_pk_fma_f16 v117, v121, s58, s58\n"
    "v_pk_fma_f16 v118, v122, s58, s58\n"
    "v_pk_fma_f16 v119, v123, s58, s58\n"
    "v_rcp_f16_e32 v120, v116\n"
    "v_rcp_f16_e32 v121, v117\n"
    "v_smfmac_f32_16x16x64_bf16 v[156:159], a[252:255], v[140:147], v240\n"
    "v_rcp_f16_e32 v122, v118\n"
    "v_rcp_f16_e32 v123, v119\n"
    "v_mfma_f32_32x32x16_f16 v[20:35], v[128:131], v[108:111], v[20:35]\n"
    "ds_read_b128 v[128:131], v244 offset:16384\n"
    "v_rcp_f16_sdwa v120, v116 dst_sel:WORD_1 dst_unused:UNUSED_PRESERVE src0_sel:WORD_1\n"
    "v_rcp_f16_sdwa v121, v117 dst_sel:WORD_1 dst_unused:UNUSED_PRESERVE src0_sel:WORD_1\n"
    "v_rcp_f16_sdwa v122, v118 dst_sel:WORD_1 dst_unused:UNUSED_PRESERVE src0_sel:WORD_1\n"
    "v_rcp_f16_sdwa v123, v119 dst_sel:WORD_1 dst_unused:UNUSED_PRESERVE src0_sel:WORD_1\n"
    "v_mfma_f32_32x32x16_f16 v[36:51], v[132:135], v[108:111], v[36:51]\n"
    "s_waitcnt lgkmcnt(2)\n"
    "ds_read_b128 v[132:135], v244 offset:17408\n"
    "v_pk_mul_f16 v112, v112, v120\n"
    "v_pk_mul_f16 v113, v113, v121\n"
    "v_pk_mul_f16 v114, v114, v122\n"
    "v_pk_mul_f16 v115, v115, v123\n"
    "v_smfmac_f32_16x16x64_bf16 v[156:159], v[208:211], v[148:155], v240\n"
    "s_waitcnt lgkmcnt(2)\n"
    "v_cvt_pk_f16_f32 v108, v68, v69\n"
    "v_cvt_pk_f16_f32 v109, v70, v71\n"
    "v_mfma_f32_32x32x16_f16 v[4:19], v[124:127], v[112:115], v[4:19]\n"
    "ds_read_b128 v[136:139], v241 offset:1408\n"
    "v_cvt_pk_f16_f32 v110, v72, v73\n"
    "v_cvt_pk_f16_f32 v111, v74, v75\n"
    "v_pk_fma_f16 v116, v108, v108, s57\n"
    "v_pk_fma_f16 v117, v109, v109, s57\n"
    "v_cvt_pk_bf16_f32 v162, v156, v157\n"
    "v_cvt_pk_bf16_f32 v163, v158, v159\n"
    "v_pk_fma_f16 v118, v110, v110, s57\n"
    "v_mfma_f32_32x32x16_bf16 v[52:67], a[72:75], v[84:87], 0\n"
    "s_waitcnt lgkmcnt(2)\n"
    "ds_read_b128 v[140:143], v241 offset:1344\n"
    "v_pk_fma_f16 v119, v111, v111, s57\n"
    "v_pk_mul_f16 v116, v116, v108\n"
    "v_pk_mul_f16 v117, v117, v109\n"
    "v_pk_mul_f16 v118, v118, v110\n"
    "v_pk_mul_f16 v119, v119, v111\n"
    "v_exp_f16_e32 v120, v116\n"
    "v_mfma_f32_32x32x16_f16 v[20:35], v[128:131], v[112:115], v[20:35]\n"
    "ds_write_b64 v242, v[162:163] offset:24\n"
    "ds_read_b128 v[144:147], v241 offset:1360\n"
    "v_exp_f16_e32 v121, v117\n"
    "v_exp_f16_e32 v122, v118\n"
    "v_exp_f16_e32 v123, v119\n"
    "v_exp_f16_sdwa v120, v116 dst_sel:WORD_1 dst_unused:UNUSED_PRESERVE src0_sel:WORD_1\n"
    "v_mfma_f32_32x32x16_bf16 v[52:67], a[76:79], v[88:91], v[52:67]\n"
    "s_waitcnt lgkmcnt(4)\n"
    "ds_read_b128 v[148:151], v241 offset:1376\n"
    "v_exp_f16_sdwa v121, v117 dst_sel:WORD_1 dst_unused:UNUSED_PRESERVE src0_sel:WORD_1\n"
    "v_exp_f16_sdwa v122, v118 dst_sel:WORD_1 dst_unused:UNUSED_PRESERVE src0_sel:WORD_1\n"
    "v_exp_f16_sdwa v123, v119 dst_sel:WORD_1 dst_unused:UNUSED_PRESERVE src0_sel:WORD_1\n"
    "v_mfma_f32_32x32x16_f16 v[36:51], v[132:135], v[112:115], v[36:51]\n"
    "ds_read_b128 v[152:155], v241 offset:1392\n"
    "v_pk_fma_f16 v116, v120, s58, s58\n"
    "v_pk_fma_f16 v117, v121, s58, s58\n"
    "v_pk_fma_f16 v118, v122, s58, s58\n"
    "v_pk_fma_f16 v119, v123, s58, s58\n"
    "v_rcp_f16_e32 v120, v116\n"
    "v_mfma_f32_32x32x16_bf16 v[52:67], a[80:83], v[92:95], v[52:67]\n"
    "ds_read_b128 v[124:127], v244 offset:18432\n"
    "v_rcp_f16_e32 v121, v117\n"
    "v_rcp_f16_e32 v122, v118\n"
    "v_rcp_f16_e32 v123, v119\n"
    "v_rcp_f16_sdwa v120, v116 dst_sel:WORD_1 dst_unused:UNUSED_PRESERVE src0_sel:WORD_1\n"
    "v_mfma_f32_32x32x16_bf16 v[52:67], a[84:87], v[96:99], v[52:67]\n"
    "s_waitcnt lgkmcnt(6)\n"
    "ds_read_b128 v[128:131], v244 offset:19456\n"
    "v_rcp_f16_sdwa v121, v117 dst_sel:WORD_1 dst_unused:UNUSED_PRESERVE src0_sel:WORD_1\n"
    "v_rcp_f16_sdwa v122, v118 dst_sel:WORD_1 dst_unused:UNUSED_PRESERVE src0_sel:WORD_1\n"
    "v_rcp_f16_sdwa v123, v119 dst_sel:WORD_1 dst_unused:UNUSED_PRESERVE src0_sel:WORD_1\n"
    "v_mfma_f32_16x16x32_bf16 v[156:159], v[224:227], v[136:139], 0\n"
    "ds_read_b128 v[132:135], v244 offset:20480\n"
    "v_pk_mul_f16 v108, v108, v120\n"
    "v_pk_mul_f16 v109, v109, v121\n"
    "v_pk_mul_f16 v110, v110, v122\n"
    "v_pk_mul_f16 v111, v111, v123\n"
    "v_cvt_pk_f16_f32 v112, v76, v77\n"
    "v_cvt_pk_f16_f32 v113, v78, v79\n"
    "v_cvt_pk_f16_f32 v114, v80, v81\n"
    "v_mfma_f32_32x32x16_bf16 v[52:67], a[88:91], v[100:103], v[52:67]\n"
    "s_waitcnt lgkmcnt(5)\n"
    "v_cvt_pk_f16_f32 v115, v82, v83\n"
    "v_pk_fma_f16 v116, v112, v112, s57\n"
    "v_pk_fma_f16 v117, v113, v113, s57\n"
    "v_pk_fma_f16 v118, v114, v114, s57\n"
    "v_pk_fma_f16 v119, v115, v115, s57\n"
    "v_pk_mul_f16 v116, v116, v112\n"
    "v_pk_mul_f16 v117, v117, v113\n"
    "v_smfmac_f32_16x16x64_bf16 v[156:159], v[216:219], v[140:147], v240\n"
    "v_pk_mul_f16 v118, v118, v114\n"
    "v_pk_mul_f16 v119, v119, v115\n"
    "v_exp_f16_e32 v120, v116\n"
    "v_exp_f16_e32 v121, v117\n"
    "v_mfma_f32_32x32x16_bf16 v[52:67], a[92:95], v[104:107], v[52:67]\n"
    "s_waitcnt lgkmcnt(2)\n"
    "v_exp_f16_e32 v122, v118\n"
    "v_exp_f16_e32 v123, v119\n"
    "v_exp_f16_sdwa v120, v116 dst_sel:WORD_1 dst_unused:UNUSED_PRESERVE src0_sel:WORD_1\n"
    "v_mfma_f32_32x32x16_f16 v[4:19], v[124:127], v[108:111], v[4:19]\n"
    "ds_read_b128 v[124:127], v244 offset:21504\n"
    "v_exp_f16_sdwa v121, v117 dst_sel:WORD_1 dst_unused:UNUSED_PRESERVE src0_sel:WORD_1\n"
    "v_exp_f16_sdwa v122, v118 dst_sel:WORD_1 dst_unused:UNUSED_PRESERVE src0_sel:WORD_1\n"
    "v_exp_f16_sdwa v123, v119 dst_sel:WORD_1 dst_unused:UNUSED_PRESERVE src0_sel:WORD_1\n"
    "v_smfmac_f32_16x16x64_bf16 v[156:159], v[220:223], v[148:155], v240\n"
    "s_waitcnt lgkmcnt(2)\n"
    "v_pk_fma_f16 v116, v120, s58, s58\n"
    "v_pk_fma_f16 v117, v121, s58, s58\n"
    "v_pk_fma_f16 v118, v122, s58, s58\n"
    "v_pk_fma_f16 v119, v123, s58, s58\n"
    "v_rcp_f16_e32 v120, v116\n"
    "v_rcp_f16_e32 v121, v117\n"
    "v_mfma_f32_32x32x16_f16 v[20:35], v[128:131], v[108:111], v[20:35]\n"
    "s_waitcnt lgkmcnt(1)\n"
    "ds_read_b128 v[128:131], v244 offset:22528\n"
    "v_cvt_pk_bf16_f32 v160, v156, v157\n"
    "v_cvt_pk_bf16_f32 v161, v158, v159\n"
    "v_rcp_f16_e32 v122, v118\n"
    "v_rcp_f16_e32 v123, v119\n"
    "v_rcp_f16_sdwa v120, v116 dst_sel:WORD_1 dst_unused:UNUSED_PRESERVE src0_sel:WORD_1\n"
    "v_rcp_f16_sdwa v121, v117 dst_sel:WORD_1 dst_unused:UNUSED_PRESERVE src0_sel:WORD_1\n"
    "v_rcp_f16_sdwa v122, v118 dst_sel:WORD_1 dst_unused:UNUSED_PRESERVE src0_sel:WORD_1\n"
    "v_rcp_f16_sdwa v123, v119 dst_sel:WORD_1 dst_unused:UNUSED_PRESERVE src0_sel:WORD_1\n"
    "v_mfma_f32_32x32x16_f16 v[36:51], v[132:135], v[108:111], v[36:51]\n"
    "s_waitcnt lgkmcnt(1)\n"
    "ds_read_b128 v[132:135], v244 offset:23552\n"
    "ds_write_b64 v242, v[160:161] offset:32\n"
    "v_pk_mul_f16 v112, v112, v120\n"
    "v_pk_mul_f16 v113, v113, v121\n"
    "v_pk_mul_f16 v114, v114, v122\n"
    "v_pk_mul_f16 v115, v115, v123\n"
    "v_cvt_pk_f16_f32 v108, v52, v53\n"
    "v_cvt_pk_f16_f32 v109, v54, v55\n"
    "v_mfma_f32_32x32x16_f16 v[4:19], v[124:127], v[112:115], v[4:19]\n"
    "ds_read_b128 v[136:139], v241 offset:1744\n"
    "v_cvt_pk_f16_f32 v110, v56, v57\n"
    "v_cvt_pk_f16_f32 v111, v58, v59\n"
    "v_pk_fma_f16 v116, v108, v108, s57\n"
    "v_pk_fma_f16 v117, v109, v109, s57\n"
    "v_pk_fma_f16 v118, v110, v110, s57\n"
    "v_mfma_f32_32x32x16_bf16 v[68:83], a[96:99], v[84:87], 0\n"
    "s_waitcnt lgkmcnt(3)\n"
    "ds_read_b128 v[140:143], v241 offset:1680\n"
    "v_pk_fma_f16 v119, v111, v111, s57\n"
    "v_pk_mul_f16 v116, v116, v108\n"
    "v_pk_mul_f16 v117, v117, v109\n"
    "v_pk_mul_f16 v118, v118, v110\n"
    "v_pk_mul_f16 v119, v119, v111\n"
    "v_exp_f16_e32 v120, v116\n"
    "v_mfma_f32_32x32x16_f16 v[20:35], v[128:131], v[112:115], v[20:35]\n"
    "ds_read_b128 v[144:147], v241 offset:1696\n"
    "v_exp_f16_e32 v121, v117\n"
    "v_exp_f16_e32 v122, v118\n"
    "v_exp_f16_e32 v123, v119\n"
    "v_exp_f16_sdwa v120, v116 dst_sel:WORD_1 dst_unused:UNUSED_PRESERVE src0_sel:WORD_1\n"
    "v_mfma_f32_32x32x16_bf16 v[68:83], a[100:103], v[88:91], v[68:83]\n"
    "s_waitcnt lgkmcnt(4)\n"
    "ds_read_b128 v[148:151], v241 offset:1712\n"
    "v_exp_f16_sdwa v121, v117 dst_sel:WORD_1 dst_unused:UNUSED_PRESERVE src0_sel:WORD_1\n"
    "v_exp_f16_sdwa v122, v118 dst_sel:WORD_1 dst_unused:UNUSED_PRESERVE src0_sel:WORD_1\n"
    "v_exp_f16_sdwa v123, v119 dst_sel:WORD_1 dst_unused:UNUSED_PRESERVE src0_sel:WORD_1\n"
    "v_mfma_f32_32x32x16_f16 v[36:51], v[132:135], v[112:115], v[36:51]\n"
    "s_waitcnt vmcnt(3)\n"
    "ds_read_b128 v[152:155], v241 offset:1728\n"
    "ds_write_b64 v246, v[164:165]\n"
    "ds_write_b64 v246, v[166:167] offset:336\n"
    "v_pk_fma_f16 v116, v120, s58, s58\n"
    "v_pk_fma_f16 v117, v121, s58, s58\n"
    "v_pk_fma_f16 v118, v122, s58, s58\n"
    "v_pk_fma_f16 v119, v123, s58, s58\n"
    "v_rcp_f16_e32 v120, v116\n"
    "v_mfma_f32_32x32x16_bf16 v[68:83], a[104:107], v[92:95], v[68:83]\n"
    "s_waitcnt vmcnt(2)\n"
    "ds_read_b128 v[124:127], v244 offset:24576\n"
    "ds_write_b64 v247, v[168:169]\n"
    "v_rcp_f16_e32 v121, v117\n"
    "v_rcp_f16_e32 v122, v118\n"
    "v_rcp_f16_e32 v123, v119\n"
    "v_rcp_f16_sdwa v120, v116 dst_sel:WORD_1 dst_unused:UNUSED_PRESERVE src0_sel:WORD_1\n"
    "v_mfma_f32_32x32x16_bf16 v[68:83], a[108:111], v[96:99], v[68:83]\n"
    "s_waitcnt lgkmcnt(8)\n"
    "ds_read_b128 v[128:131], v244 offset:25600\n"
    "ds_write_b64 v247, v[170:171] offset:336\n"
    "v_rcp_f16_sdwa v121, v117 dst_sel:WORD_1 dst_unused:UNUSED_PRESERVE src0_sel:WORD_1\n"
    "v_rcp_f16_sdwa v122, v118 dst_sel:WORD_1 dst_unused:UNUSED_PRESERVE src0_sel:WORD_1\n"
    "v_rcp_f16_sdwa v123, v119 dst_sel:WORD_1 dst_unused:UNUSED_PRESERVE src0_sel:WORD_1\n"
    "v_mfma_f32_16x16x32_bf16 v[156:159], v[236:239], v[136:139], 0\n"
    "ds_read_b128 v[132:135], v244 offset:26624\n"
    "v_pk_mul_f16 v108, v108, v120\n"
    "v_pk_mul_f16 v109, v109, v121\n"
    "v_pk_mul_f16 v110, v110, v122\n"
    "v_pk_mul_f16 v111, v111, v123\n"
    "v_cvt_pk_f16_f32 v112, v60, v61\n"
    "v_cvt_pk_f16_f32 v113, v62, v63\n"
    "v_cvt_pk_f16_f32 v114, v64, v65\n"
    "v_mfma_f32_32x32x16_bf16 v[68:83], a[112:115], v[100:103], v[68:83]\n"
    "s_waitcnt vmcnt(1) lgkmcnt(9)\n"
    "ds_write_b64 v248, v[172:173]\n"
    "v_cvt_pk_f16_f32 v115, v66, v67\n"
    "v_pk_fma_f16 v116, v112, v112, s57\n"
    "v_pk_fma_f16 v117, v113, v113, s57\n"
    "v_pk_fma_f16 v118, v114, v114, s57\n"
    "v_pk_fma_f16 v119, v115, v115, s57\n"
    "v_pk_mul_f16 v116, v116, v112\n"
    "v_pk_mul_f16 v117, v117, v113\n"
    "v_smfmac_f32_16x16x64_bf16 v[156:159], v[228:231], v[140:147], v240\n"
    "ds_write_b64 v248, v[174:175] offset:336\n"
    "v_pk_mul_f16 v118, v118, v114\n"
    "v_pk_mul_f16 v119, v119, v115\n"
    "v_exp_f16_e32 v120, v116\n"
    "v_exp_f16_e32 v121, v117\n"
    "v_mfma_f32_32x32x16_bf16 v[68:83], a[116:119], v[104:107], v[68:83]\n"
    "s_waitcnt vmcnt(0) lgkmcnt(6)\n"
    "ds_write_b64 v249, v[176:177]\n"
    "v_exp_f16_e32 v122, v118\n"
    "v_exp_f16_e32 v123, v119\n"
    "v_exp_f16_sdwa v120, v116 dst_sel:WORD_1 dst_unused:UNUSED_PRESERVE src0_sel:WORD_1\n"
    "v_mfma_f32_32x32x16_f16 v[4:19], v[124:127], v[108:111], v[4:19]\n"
    "ds_read_b128 v[124:127], v244 offset:27648\n"
    "v_exp_f16_sdwa v121, v117 dst_sel:WORD_1 dst_unused:UNUSED_PRESERVE src0_sel:WORD_1\n"
    "v_exp_f16_sdwa v122, v118 dst_sel:WORD_1 dst_unused:UNUSED_PRESERVE src0_sel:WORD_1\n"
    "v_exp_f16_sdwa v123, v119 dst_sel:WORD_1 dst_unused:UNUSED_PRESERVE src0_sel:WORD_1\n"
    "v_smfmac_f32_16x16x64_bf16 v[156:159], v[232:235], v[148:155], v240\n"
    "s_waitcnt lgkmcnt(6)\n"
    "ds_write_b64 v249, v[178:179] offset:336\n"
    "s_add_u32 s70, s54, 0x4000\n"
    "s_cmp_ge_u32 s70, 0xc000\n"
    "s_cselect_b32 s53, s76, s77\n"
    "s_add_u32 s54, s54, s53\n"
    "v_add_u32 v246, s53, v246\n"
    "v_pk_fma_f16 v116, v120, s58, s58\n"
    "v_add_u32 v247, s53, v247\n"
    "v_add_u32 v248, s53, v248\n"
    "v_add_u32 v249, s53, v249\n"
    "v_pk_fma_f16 v117, v121, s58, s58\n"
    "v_pk_fma_f16 v118, v122, s58, s58\n"
    "v_pk_fma_f16 v119, v123, s58, s58\n"
    "v_rcp_f16_e32 v120, v116\n"
    "v_rcp_f16_e32 v121, v117\n"
    "v_mfma_f32_32x32x16_f16 v[20:35], v[128:131], v[108:111], v[20:35]\n"
    "s_waitcnt lgkmcnt(5)\n"
    "ds_read_b128 v[128:131], v244 offset:28672\n"
    "v_cvt_pk_bf16_f32 v162, v156, v157\n"
    "v_cvt_pk_bf16_f32 v163, v158, v159\n"
    "v_rcp_f16_e32 v122, v118\n"
    "v_rcp_f16_e32 v123, v119\n"
    "v_rcp_f16_sdwa v120, v116 dst_sel:WORD_1 dst_unused:UNUSED_PRESERVE src0_sel:WORD_1\n"
    "v_rcp_f16_sdwa v121, v117 dst_sel:WORD_1 dst_unused:UNUSED_PRESERVE src0_sel:WORD_1\n"
    "v_add_u32 v241, 0x4000, v241\n"
    "v_add_u32 v254, 0xffff4000, v241\n"
    "v_min_u32 v241, v241, v254\n"
    "v_rcp_f16_sdwa v122, v118 dst_sel:WORD_1 dst_unused:UNUSED_PRESERVE src0_sel:WORD_1\n"
    "v_rcp_f16_sdwa v123, v119 dst_sel:WORD_1 dst_unused:UNUSED_PRESERVE src0_sel:WORD_1\n"
    "v_mfma_f32_32x32x16_f16 v[36:51], v[132:135], v[108:111], v[36:51]\n"
    "ds_read_b128 v[132:135], v244 offset:29696\n"
    "ds_write_b64 v242, v[162:163] offset:40\n"
    "v_pk_mul_f16 v112, v112, v120\n"
    "v_pk_mul_f16 v113, v113, v121\n"
    "v_pk_mul_f16 v114, v114, v122\n"
    "v_pk_mul_f16 v115, v115, v123\n"
    "s_waitcnt lgkmcnt(3)\n"
    "s_barrier\n"
    "v_cvt_pk_f16_f32 v108, v68, v69\n"
    "v_cvt_pk_f16_f32 v109, v70, v71\n"
    "v_mfma_f32_32x32x16_f16 v[4:19], v[124:127], v[112:115], v[4:19]\n"
    "ds_read_b128 v[136:139], v241 offset:64\n"
    "v_cvt_pk_f16_f32 v110, v72, v73\n"
    "v_cvt_pk_f16_f32 v111, v74, v75\n"
    "v_pk_fma_f16 v116, v108, v108, s57\n"
    "v_pk_fma_f16 v117, v109, v109, s57\n"
    "v_pk_fma_f16 v118, v110, v110, s57\n"
    "v_mfma_f32_32x32x16_bf16 v[52:67], a[120:123], v[84:87], 0\n"
    "s_waitcnt lgkmcnt(3)\n"
    "ds_read_b128 v[140:143], v241\n"
    "v_pk_fma_f16 v119, v111, v111, s57\n"
    "v_pk_mul_f16 v116, v116, v108\n"
    "v_pk_mul_f16 v117, v117, v109\n"
    "v_pk_mul_f16 v118, v118, v110\n"
    "v_pk_mul_f16 v119, v119, v111\n"
    "v_exp_f16_e32 v120, v116\n"
    "v_mfma_f32_32x32x16_f16 v[20:35], v[128:131], v[112:115], v[20:35]\n"
    "ds_read_b128 v[144:147], v241 offset:16\n"
    "v_exp_f16_e32 v121, v117\n"
    "v_exp_f16_e32 v122, v118\n"
    "v_exp_f16_e32 v123, v119\n"
    "v_exp_f16_sdwa v120, v116 dst_sel:WORD_1 dst_unused:UNUSED_PRESERVE src0_sel:WORD_1\n"
    "v_mfma_f32_32x32x16_bf16 v[52:67], a[124:127], v[88:91], v[52:67]\n"
    "s_waitcnt lgkmcnt(4)\n"
    "ds_read_b128 v[148:151], v241 offset:32\n"
    "v_exp_f16_sdwa v121, v117 dst_sel:WORD_1 dst_unused:UNUSED_PRESERVE src0_sel:WORD_1\n"
    "v_exp_f16_sdwa v122, v118 dst_sel:WORD_1 dst_unused:UNUSED_PRESERVE src0_sel:WORD_1\n"
    "v_exp_f16_sdwa v123, v119 dst_sel:WORD_1 dst_unused:UNUSED_PRESERVE src0_sel:WORD_1\n"
    "v_mfma_f32_32x32x16_f16 v[36:51], v[132:135], v[112:115], v[36:51]\n"
    "ds_read_b128 v[152:155], v241 offset:48\n"
    "v_pk_fma_f16 v116, v120, s58, s58\n"
    "v_pk_fma_f16 v117, v121, s58, s58\n"
    "v_pk_fma_f16 v118, v122, s58, s58\n"
    "v_pk_fma_f16 v119, v123, s58, s58\n"
    "v_rcp_f16_e32 v120, v116\n"
    "v_mfma_f32_32x32x16_bf16 v[52:67], a[128:131], v[92:95], v[52:67]\n"
    "ds_read_b128 v[124:127], v244 offset:30720\n"
    "v_rcp_f16_e32 v121, v117\n"
    "v_rcp_f16_e32 v122, v118\n"
    "v_rcp_f16_e32 v123, v119\n"
    "v_rcp_f16_sdwa v120, v116 dst_sel:WORD_1 dst_unused:UNUSED_PRESERVE src0_sel:WORD_1\n"
    "v_mfma_f32_32x32x16_bf16 v[52:67], a[132:135], v[96:99], v[52:67]\n"
    "s_waitcnt lgkmcnt(5)\n"
    "ds_read_b128 v[128:131], v244 offset:31744\n"
    "buffer_load_dwordx4 v[180:183], v253, s[36:39], s56 offen offset:128\n"
    "v_rcp_f16_sdwa v121, v117 dst_sel:WORD_1 dst_unused:UNUSED_PRESERVE src0_sel:WORD_1\n"
    "v_rcp_f16_sdwa v122, v118 dst_sel:WORD_1 dst_unused:UNUSED_PRESERVE src0_sel:WORD_1\n"
    "v_rcp_f16_sdwa v123, v119 dst_sel:WORD_1 dst_unused:UNUSED_PRESERVE src0_sel:WORD_1\n"
    "v_mfma_f32_16x16x32_bf16 v[156:159], v[188:191], v[136:139], 0\n"
    "ds_read_b128 v[132:135], v244 offset:32768\n"
    "v_pk_mul_f16 v108, v108, v120\n"
    "v_pk_mul_f16 v109, v109, v121\n"
    "v_pk_mul_f16 v110, v110, v122\n"
    "v_pk_mul_f16 v111, v111, v123\n"
    "v_cvt_pk_f16_f32 v112, v76, v77\n"
    "v_cvt_pk_f16_f32 v113, v78, v79\n"
    "v_cvt_pk_f16_f32 v114, v80, v81\n"
    "v_mfma_f32_32x32x16_bf16 v[52:67], a[136:139], v[100:103], v[52:67]\n"
    "s_waitcnt lgkmcnt(5)\n"
    "buffer_load_dwordx4 v[164:167], v250, s[36:39], s52 offen\n"
    "v_cvt_pk_f16_f32 v115, v82, v83\n"
    "v_pk_fma_f16 v116, v112, v112, s57\n"
    "v_pk_fma_f16 v117, v113, v113, s57\n"
    "v_pk_fma_f16 v118, v114, v114, s57\n"
    "v_pk_fma_f16 v119, v115, v115, s57\n"
    "v_pk_mul_f16 v116, v116, v112\n"
    "v_pk_mul_f16 v117, v117, v113\n"
    "v_smfmac_f32_16x16x64_bf16 v[156:159], a[240:243], v[140:147], v240\n"
    "buffer_load_dwordx4 v[168:171], v251, s[36:39], s52 offen\n"
    "v_pk_mul_f16 v118, v118, v114\n"
    "v_pk_mul_f16 v119, v119, v115\n"
    "v_exp_f16_e32 v120, v116\n"
    "v_exp_f16_e32 v121, v117\n"
    "v_mfma_f32_32x32x16_bf16 v[52:67], a[140:143], v[104:107], v[52:67]\n"
    "s_waitcnt lgkmcnt(2)\n"
    "buffer_load_dwordx4 v[172:175], v252, s[36:39], s52 offen\n"
    "v_exp_f16_e32 v122, v118\n"
    "v_exp_f16_e32 v123, v119\n"
    "v_exp_f16_sdwa v120, v116 dst_sel:WORD_1 dst_unused:UNUSED_PRESERVE src0_sel:WORD_1\n"
    "v_mfma_f32_32x32x16_f16 v[4:19], v[124:127], v[108:111], v[4:19]\n"
    "ds_read_b128 v[124:127], v244 offset:33792\n"
    "v_exp_f16_sdwa v121, v117 dst_sel:WORD_1 dst_unused:UNUSED_PRESERVE src0_sel:WORD_1\n"
    "v_exp_f16_sdwa v122, v118 dst_sel:WORD_1 dst_unused:UNUSED_PRESERVE src0_sel:WORD_1\n"
    "v_exp_f16_sdwa v123, v119 dst_sel:WORD_1 dst_unused:UNUSED_PRESERVE src0_sel:WORD_1\n"
    "v_smfmac_f32_16x16x64_bf16 v[156:159], v[184:187], v[148:155], v240\n"
    "s_waitcnt lgkmcnt(2)\n"
    "buffer_load_dwordx4 v[176:179], v1, s[36:39], s52 offen\n"
    "s_add_u32 s52, s52, 393216\n"
    "v_pk_fma_f16 v116, v120, s58, s58\n"
    "v_pk_fma_f16 v117, v121, s58, s58\n"
    "v_pk_fma_f16 v118, v122, s58, s58\n"
    "v_pk_fma_f16 v119, v123, s58, s58\n"
    "v_rcp_f16_e32 v120, v116\n"
    "v_rcp_f16_e32 v121, v117\n"
    "v_mfma_f32_32x32x16_f16 v[20:35], v[128:131], v[108:111], v[20:35]\n"
    "s_waitcnt lgkmcnt(1)\n"
    "ds_read_b128 v[128:131], v244 offset:34816\n"
    "v_cvt_pk_bf16_f32 v160, v156, v157\n"
    "v_cvt_pk_bf16_f32 v161, v158, v159\n"
    "v_rcp_f16_e32 v122, v118\n"
    "v_rcp_f16_e32 v123, v119\n"
    "v_rcp_f16_sdwa v120, v116 dst_sel:WORD_1 dst_unused:UNUSED_PRESERVE src0_sel:WORD_1\n"
    "v_rcp_f16_sdwa v121, v117 dst_sel:WORD_1 dst_unused:UNUSED_PRESERVE src0_sel:WORD_1\n"
    "v_rcp_f16_sdwa v122, v118 dst_sel:WORD_1 dst_unused:UNUSED_PRESERVE src0_sel:WORD_1\n"
    "v_rcp_f16_sdwa v123, v119 dst_sel:WORD_1 dst_unused:UNUSED_PRESERVE src0_sel:WORD_1\n"
    "v_mfma_f32_32x32x16_f16 v[36:51], v[132:135], v[108:111], v[36:51]\n"
    "s_waitcnt lgkmcnt(1)\n"
    "ds_read_b128 v[132:135], v244 offset:35840\n"
    "ds_write_b64 v242, v[160:161] offset:13312\n"
    "v_pk_mul_f16 v112, v112, v120\n"
    "v_pk_mul_f16 v113, v113, v121\n"
    "v_pk_mul_f16 v114, v114, v122\n"
    "v_pk_mul_f16 v115, v115, v123\n"
    "v_cvt_pk_f16_f32 v108, v52, v53\n"
    "v_cvt_pk_f16_f32 v109, v54, v55\n"
    "v_mfma_f32_32x32x16_f16 v[4:19], v[124:127], v[112:115], v[4:19]\n"
    "ds_read_b128 v[136:139], v241 offset:400\n"
    "v_cvt_pk_f16_f32 v110, v56, v57\n"
    "v_cvt_pk_f16_f32 v111, v58, v59\n"
    "v_pk_fma_f16 v116, v108, v108, s57\n"
    "v_pk_fma_f16 v117, v109, v109, s57\n"
    "v_pk_fma_f16 v118, v110, v110, s57\n"
    "v_mfma_f32_32x32x16_bf16 v[68:83], a[144:147], v[84:87], 0\n"
    "s_waitcnt lgkmcnt(3)\n"
    "ds_read_b128 v[140:143], v241 offset:336\n"
    "v_pk_fma_f16 v119, v111, v111, s57\n"
    "v_pk_mul_f16 v116, v116, v108\n"
    "v_pk_mul_f16 v117, v117, v109\n"
    "v_pk_mul_f16 v118, v118, v110\n"
    "v_pk_mul_f16 v119, v119, v111\n"
    "v_exp_f16_e32 v120, v116\n"
    "v_mfma_f32_32x32x16_f16 v[20:35], v[128:131], v[112:115], v[20:35]\n"
    "ds_read_b128 v[144:147], v241 offset:352\n"
    "v_exp_f16_e32 v121, v117\n"
    "v_exp_f16_e32 v122, v118\n"
    "v_exp_f16_e32 v123, v119\n"
    "v_exp_f16_sdwa v120, v116 dst_sel:WORD_1 dst_unused:UNUSED_PRESERVE src0_sel:WORD_1\n"
    "v_mfma_f32_32x32x16_bf16 v[68:83], a[148:151], v[88:91], v[68:83]\n"
    "s_waitcnt lgkmcnt(4)\n"
    "ds_read_b128 v[148:151], v241 offset:368\n"
    "v_exp_f16_sdwa v121, v117 dst_sel:WORD_1 dst_unused:UNUSED_PRESERVE src0_sel:WORD_1\n"
    "v_exp_f16_sdwa v122, v118 dst_sel:WORD_1 dst_unused:UNUSED_PRESERVE src0_sel:WORD_1\n"
    "v_exp_f16_sdwa v123, v119 dst_sel:WORD_1 dst_unused:UNUSED_PRESERVE src0_sel:WORD_1\n"
    "v_mfma_f32_32x32x16_f16 v[36:51], v[132:135], v[112:115], v[36:51]\n"
    "ds_read_b128 v[152:155], v241 offset:384\n"
    "v_pk_fma_f16 v116, v120, s58, s58\n"
    "v_pk_fma_f16 v117, v121, s58, s58\n"
    "v_pk_fma_f16 v118, v122, s58, s58\n"
    "v_pk_fma_f16 v119, v123, s58, s58\n"
    "v_rcp_f16_e32 v120, v116\n"
    "v_mfma_f32_32x32x16_bf16 v[68:83], a[152:155], v[92:95], v[68:83]\n"
    "ds_read_b128 v[124:127], v244 offset:36864\n"
    "v_rcp_f16_e32 v121, v117\n"
    "v_rcp_f16_e32 v122, v118\n"
    "v_rcp_f16_e32 v123, v119\n"
    "v_rcp_f16_sdwa v120, v116 dst_sel:WORD_1 dst_unused:UNUSED_PRESERVE src0_sel:WORD_1\n"
    "v_mfma_f32_32x32x16_bf16 v[68:83], a[156:159], v[96:99], v[68:83]\n"
    "s_waitcnt lgkmcnt(5)\n"
    "ds_read_b128 v[128:131], v244 offset:37888\n"
    "v_rcp_f16_sdwa v121, v117 dst_sel:WORD_1 dst_unused:UNUSED_PRESERVE src0_sel:WORD_1\n"
    "v_rcp_f16_sdwa v122, v118 dst_sel:WORD_1 dst_unused:UNUSED_PRESERVE src0_sel:WORD_1\n"
    "v_rcp_f16_sdwa v123, v119 dst_sel:WORD_1 dst_unused:UNUSED_PRESERVE src0_sel:WORD_1\n"
    "v_mfma_f32_16x16x32_bf16 v[156:159], v[196:199], v[136:139], 0\n"
    "ds_read_b128 v[132:135], v244 offset:38912\n"
    "v_pk_mul_f16 v108, v108, v120\n"
    "v_pk_mul_f16 v109, v109, v121\n"
    "v_pk_mul_f16 v110, v110, v122\n"
    "v_pk_mul_f16 v111, v111, v123\n"
    "v_cvt_pk_f16_f32 v112, v60, v61\n"
    "v_cvt_pk_f16_f32 v113, v62, v63\n"
    "v_cvt_pk_f16_f32 v114, v64, v65\n"
    "v_mfma_f32_32x32x16_bf16 v[68:83], a[160:163], v[100:103], v[68:83]\n"
    "s_waitcnt lgkmcnt(5)\n"
    "v_cvt_pk_f16_f32 v115, v66, v67\n"
    "v_pk_fma_f16 v116, v112, v112, s57\n"
    "v_pk_fma_f16 v117, v113, v113, s57\n"
    "v_pk_fma_f16 v118, v114, v114, s57\n"
    "v_pk_fma_f16 v119, v115, v115, s57\n"
    "v_pk_mul_f16 v116, v116, v112\n"
    "v_pk_mul_f16 v117, v117, v113\n"
    "v_smfmac_f32_16x16x64_bf16 v[156:159], a[244:247], v[140:147], v240\n"
    "v_pk_mul_f16 v118, v118, v114\n"
    "v_pk_mul_f16 v119, v119, v115\n"
    "v_exp_f16_e32 v120, v116\n"
    "v_exp_f16_e32 v121, v117\n"
    "v_mfma_f32_32x32x16_bf16 v[68:83], a[164:167], v[104:107], v[68:83]\n"
    "s_waitcnt lgkmcnt(2)\n"
    "v_exp_f16_e32 v122, v118\n"
    "v_exp_f16_e32 v123, v119\n"
    "v_exp_f16_sdwa v120, v116 dst_sel:WORD_1 dst_unused:UNUSED_PRESERVE src0_sel:WORD_1\n"
    "v_mfma_f32_32x32x16_f16 v[4:19], v[124:127], v[108:111], v[4:19]\n"
    "ds_read_b128 v[124:127], v244 offset:39936\n"
    "v_exp_f16_sdwa v121, v117 dst_sel:WORD_1 dst_unused:UNUSED_PRESERVE src0_sel:WORD_1\n"
    "v_exp_f16_sdwa v122, v118 dst_sel:WORD_1 dst_unused:UNUSED_PRESERVE src0_sel:WORD_1\n"
    "v_exp_f16_sdwa v123, v119 dst_sel:WORD_1 dst_unused:UNUSED_PRESERVE src0_sel:WORD_1\n"
    "v_smfmac_f32_16x16x64_bf16 v[156:159], v[192:195], v[148:155], v240\n"
    "s_waitcnt lgkmcnt(2)\n"
    "v_pk_fma_f16 v116, v120, s58, s58\n"
    "v_pk_fma_f16 v117, v121, s58, s58\n"
    "v_pk_fma_f16 v118, v122, s58, s58\n"
    "v_pk_fma_f16 v119, v123, s58, s58\n"
    "v_rcp_f16_e32 v120, v116\n"
    "v_rcp_f16_e32 v121, v117\n"
    "v_mfma_f32_32x32x16_f16 v[20:35], v[128:131], v[108:111], v[20:35]\n"
    "s_waitcnt lgkmcnt(1)\n"
    "ds_read_b128 v[128:131], v244 offset:40960\n"
    "v_cvt_pk_bf16_f32 v162, v156, v157\n"
    "v_cvt_pk_bf16_f32 v163, v158, v159\n"
    "v_rcp_f16_e32 v122, v118\n"
    "v_rcp_f16_e32 v123, v119\n"
    "v_rcp_f16_sdwa v120, v116 dst_sel:WORD_1 dst_unused:UNUSED_PRESERVE src0_sel:WORD_1\n"
    "v_rcp_f16_sdwa v121, v117 dst_sel:WORD_1 dst_unused:UNUSED_PRESERVE src0_sel:WORD_1\n"
    "v_rcp_f16_sdwa v122, v118 dst_sel:WORD_1 dst_unused:UNUSED_PRESERVE src0_sel:WORD_1\n"
    "v_rcp_f16_sdwa v123, v119 dst_sel:WORD_1 dst_unused:UNUSED_PRESERVE src0_sel:WORD_1\n"
    "v_mfma_f32_32x32x16_f16 v[36:51], v[132:135], v[108:111], v[36:51]\n"
    "s_waitcnt lgkmcnt(1)\n"
    "ds_read_b128 v[132:135], v244 offset:41984\n"
    "ds_write_b64 v242, v[162:163] offset:13320\n"
    "v_pk_mul_f16 v112, v112, v120\n"
    "v_pk_mul_f16 v113, v113, v121\n"
    "v_pk_mul_f16 v114, v114, v122\n"
    "v_pk_mul_f16 v115, v115, v123\n"
    "v_cvt_pk_f16_f32 v108, v68, v69\n"
    "v_cvt_pk_f16_f32 v109, v70, v71\n"
    "v_mfma_f32_32x32x16_f16 v[4:19], v[124:127], v[112:115], v[4:19]\n"
    "ds_read_b128 v[136:139], v241 offset:736\n"
    "v_cvt_pk_f16_f32 v110, v72, v73\n"
    "v_cvt_pk_f16_f32 v111, v74, v75\n"
    "v_pk_fma_f16 v116, v108, v108, s57\n"
    "v_pk_fma_f16 v117, v109, v109, s57\n"
    "v_mfma_f32_32x32x16_bf16 v[52:67], a[168:171], v[84:87], 0\n"
    "s_waitcnt lgkmcnt(3)\n"
    "ds_read_b128 v[140:143], v241 offset:672\n"
    "v_pk_fma_f16 v118, v110, v110, s57\n"
    "v_pk_fma_f16 v119, v111, v111, s57\n"
    "v_pk_mul_f16 v116, v116, v108\n"
    "v_pk_mul_f16 v117, v117, v109\n"
    "v_pk_mul_f16 v118, v118, v110\n"
    "v_pk_mul_f16 v119, v119, v111\n"
    "v_mfma_f32_32x32x16_f16 v[20:35], v[128:131], v[112:115], v[20:35]\n"
    "ds_read_b128 v[144:147], v241 offset:688\n"
    "v_exp_f16_e32 v120, v116\n"
    "v_exp_f16_e32 v121, v117\n"
    "v_exp_f16_e32 v122, v118\n"
    "v_mfma_f32_32x32x16_bf16 v[52:67], a[172:175], v[88:91], v[52:67]\n"
    "s_waitcnt lgkmcnt(4)\n"
    "ds_read_b128 v[148:151], v241 offset:704\n"
    "v_exp_f16_e32 v123, v119\n"
    "v_exp_f16_sdwa v120, v116 dst_sel:WORD_1 dst_unused:UNUSED_PRESERVE src0_sel:WORD_1\n"
    "v_exp_f16_sdwa v121, v117 dst_sel:WORD_1 dst_unused:UNUSED_PRESERVE src0_sel:WORD_1\n"
    "v_mfma_f32_32x32x16_f16 v[36:51], v[132:135], v[112:115], v[36:51]\n"
    "ds_read_b128 v[152:155], v241 offset:720\n"
    "v_exp_f16_sdwa v122, v118 dst_sel:WORD_1 dst_unused:UNUSED_PRESERVE src0_sel:WORD_1\n"
    "v_exp_f16_sdwa v123, v119 dst_sel:WORD_1 dst_unused:UNUSED_PRESERVE src0_sel:WORD_1\n"
    "v_pk_fma_f16 v116, v120, s58, s58\n"
    "v_mfma_f32_32x32x16_bf16 v[52:67], a[176:179], v[92:95], v[52:67]\n"
    "s_waitcnt lgkmcnt(4)\n"
    "v_pk_fma_f16 v117, v121, s58, s58\n"
    "v_pk_fma_f16 v118, v122, s58, s58\n"
    "v_pk_fma_f16 v119, v123, s58, s58\n"
    "v_rcp_f16_e32 v120, v116\n"
    "v_rcp_f16_e32 v121, v117\n"
    "v_mfma_f32_16x16x32_bf16 v[156:159], v[204:207], v[136:139], 0\n"
    "s_waitcnt lgkmcnt(2)\n"
    "ds_read_b128 v[124:127], v244 offset:43008\n"
    "v_rcp_f16_e32 v122, v118\n"
    "v_rcp_f16_e32 v123, v119\n"
    "v_rcp_f16_sdwa v120, v116 dst_sel:WORD_1 dst_unused:UNUSED_PRESERVE src0_sel:WORD_1\n"
    "v_smfmac_f32_16x16x64_bf16 v[156:159], a[248:251], v[140:147], v240\n"
    "ds_read_b128 v[136:139], v241 offset:1072\n"
    "ds_read_b128 v[140:143], v241 offset:1008\n"
    "v_rcp_f16_sdwa v121, v117 dst_sel:WORD_1 dst_unused:UNUSED_PRESERVE src0_sel:WORD_1\n"
    "v_rcp_f16_sdwa v122, v118 dst_sel:WORD_1 dst_unused:UNUSED_PRESERVE src0_sel:WORD_1\n"
    "v_mfma_f32_32x32x16_bf16 v[52:67], a[180:183], v[96:99], v[52:67]\n"
    "s_waitcnt lgkmcnt(3)\n"
    "ds_read_b128 v[128:131], v244 offset:44032\n"
    "v_rcp_f16_sdwa v123, v119 dst_sel:WORD_1 dst_unused:UNUSED_PRESERVE src0_sel:WORD_1\n"
    "v_pk_mul_f16 v108, v108, v120\n"
    "v_pk_mul_f16 v109, v109, v121\n"
    "v_pk_mul_f16 v110, v110, v122\n"
    "v_pk_mul_f16 v111, v111, v123\n"
    "v_smfmac_f32_16x16x64_bf16 v[156:159], v[200:203], v[148:155], v240\n"
    "ds_read_b128 v[132:135], v244 offset:45056\n"
    "ds_read_b128 v[144:147], v241 offset:1024\n"
    "v_cvt_pk_f16_f32 v112, v76, v77\n"
    "v_cvt_pk_f16_f32 v113, v78, v79\n"
    "v_cvt_pk_f16_f32 v114, v80, v81\n"
    "v_cvt_pk_f16_f32 v115, v82, v83\n"
    "v_pk_fma_f16 v116, v112, v112, s57\n"
    "v_pk_fma_f16 v117, v113, v113, s57\n"
    "ds_read_b128 v[148:151], v241 offset:1040\n"
    "v_pk_fma_f16 v118, v114, v114, s57\n"
    "v_cvt_pk_bf16_f32 v160, v156, v157\n"
    "v_cvt_pk_bf16_f32 v161, v158, v159\n"
    "v_pk_fma_f16 v119, v115, v115, s57\n"
    "v_pk_mul_f16 v116, v116, v112\n"
    "v_pk_mul_f16 v117, v117, v113\n"
    "v_pk_mul_f16 v118, v118, v114\n"
    "v_pk_mul_f16 v119, v119, v115\n"
    "v_mfma_f32_32x32x16_bf16 v[52:67], a[184:187], v[100:103], v[52:67]\n"
    "s_waitcnt lgkmcnt(5)\n"
    "ds_write_b64 v242, v[160:161] offset:13328\n"
    "ds_read_b128 v[152:155], v241 offset:1056\n"
    "v_exp_f16_e32 v120, v116\n"
    "v_exp_f16_e32 v121, v117\n"
    "v_exp_f16_e32 v122, v118\n"
    "v_mfma_f32_16x16x32_bf16 v[156:159], v[212:215], v[136:139], 0\n"
    "v_exp_f16_e32 v123, v119\n"
    "v_exp_f16_sdwa v120, v116 dst_sel:WORD_1 dst_unused:UNUSED_PRESERVE src0_sel:WORD_1\n"
    "v_exp_f16_sdwa v121, v117 dst_sel:WORD_1 dst_unused:UNUSED_PRESERVE src0_sel:WORD_1\n"
    "v_mfma_f32_32x32x16_bf16 v[52:67], a[188:191], v[104:107], v[52:67]\n"
    "v_exp_f16_sdwa v122, v118 dst_sel:WORD_1 dst_unused:UNUSED_PRESERVE src0_sel:WORD_1\n"
    "v_exp_f16_sdwa v123, v119 dst_sel:WORD_1 dst_unused:UNUSED_PRESERVE src0_sel:WORD_1\n"
    "v_pk_fma_f16 v116, v120, s58, s58\n"
    "v_mfma_f32_32x32x16_f16 v[4:19], v[124:127], v[108:111], v[4:19]\n"
    "s_waitcnt lgkmcnt(3)\n"
    "ds_read_b128 v[124:127], v244 offset:46080\n"
    "v_pk_fma_f16 v117, v121, s58, s58\n"
    "v_pk_fma_f16 v118, v122, s58, s58\n"
    "v_pk_fma_f16 v119, v123, s58, s58\n"
    "v_rcp_f16_e32 v120, v116\n"
    "v_rcp_f16_e32 v121, v117\n"
    "v_smfmac_f32_16x16x64_bf16 v[156:159], a[252:255], v[140:147], v240\n"
    "v_rcp_f16_e32 v122, v118\n"
    "v_rcp_f16_e32 v123, v119\n"
    "v_mfma_f32_32x32x16_f16 v[20:35], v[128:131], v[108:111], v[20:35]\n"
    "ds_read_b128 v[128:131], v244 offset:47104\n"
    "v_rcp_f16_sdwa v120, v116 dst_sel:WORD_1 dst_unused:UNUSED_PRESERVE src0_sel:WORD_1\n"
    "v_rcp_f16_sdwa v121, v117 dst_sel:WORD_1 dst_unused:UNUSED_PRESERVE src0_sel:WORD_1\n"
    "v_rcp_f16_sdwa v122, v118 dst_sel:WORD_1 dst_unused:UNUSED_PRESERVE src0_sel:WORD_1\n"
    "v_rcp_f16_sdwa v123, v119 dst_sel:WORD_1 dst_unused:UNUSED_PRESERVE src0_sel:WORD_1\n"
    "v_mfma_f32_32x32x16_f16 v[36:51], v[132:135], v[108:111], v[36:51]\n"
    "s_waitcnt lgkmcnt(2)\n"
    "ds_read_b128 v[132:135], v244 offset:48128\n"
    "v_pk_mul_f16 v112, v112, v120\n"
    "v_pk_mul_f16 v113, v113, v121\n"
    "v_pk_mul_f16 v114, v114, v122\n"
    "v_pk_mul_f16 v115, v115, v123\n"
    "v_smfmac_f32_16x16x64_bf16 v[156:159], v[208:211], v[148:155], v240\n"
    "s_waitcnt lgkmcnt(2)\n"
    "v_cvt_pk_f16_f32 v108, v52, v53\n"
    "v_cvt_pk_f16_f32 v109, v54, v55\n"
    "v_mfma_f32_32x32x16_f16 v[4:19], v[124:127], v[112:115], v[4:19]\n"
    "ds_read_b128 v[136:139], v241 offset:1408\n"
    "v_cvt_pk_f16_f32 v110, v56, v57\n"
    "v_cvt_pk_f16_f32 v111, v58, v59\n"
    "v_pk_fma_f16 v116, v108, v108, s57\n"
    "v_pk_fma_f16 v117, v109, v109, s57\n"
    "v_cvt_pk_bf16_f32 v162, v156, v157\n"
    "v_cvt_pk_bf16_f32 v163, v158, v159\n"
    "v_pk_fma_f16 v118, v110, v110, s57\n"
    "v_mfma_f32_32x32x16_bf16 v[68:83], a[192:195], v[84:87], 0\n"
    "s_waitcnt lgkmcnt(2)\n"
    "ds_read_b128 v[140:143], v241 offset:1344\n"
    "v_pk_fma_f16 v119, v111, v111, s57\n"
    "v_pk_mul_f16 v116, v116, v108\n"
    "v_pk_mul_f16 v117, v117, v109\n"
    "v_pk_mul_f16 v118, v118, v110\n"
    "v_pk_mul_f16 v119, v119, v111\n"
    "v_exp_f16_e32 v120, v116\n"
    "v_mfma_f32_32x32x16_f16 v[20:35], v[128:131], v[112:115], v[20:35]\n"
    "ds_write_b64 v242, v[162:163] offset:13336\n"
    "ds_read_b128 v[144:147], v241 offset:1360\n"
    "v_exp_f16_e32 v121, v117\n"
    "v_exp_f16_e32 v122, v118\n"
    "v_exp_f16_e32 v123, v119\n"
    "v_exp_f16_sdwa v120, v116 dst_sel:WORD_1 dst_unused:UNUSED_PRESERVE src0_sel:WORD_1\n"
    "v_mfma_f32_32x32x16_bf16 v[68:83], a[196:199], v[88:91], v[68:83]\n"
    "s_waitcnt lgkmcnt(4)\n"
    "ds_read_b128 v[148:151], v241 offset:1376\n"
    "v_exp_f16_sdwa v121, v117 dst_sel:WORD_1 dst_unused:UNUSED_PRESERVE src0_sel:WORD_1\n"
    "v_exp_f16_sdwa v122, v118 dst_sel:WORD_1 dst_unused:UNUSED_PRESERVE src0_sel:WORD_1\n"
    "v_exp_f16_sdwa v123, v119 dst_sel:WORD_1 dst_unused:UNUSED_PRESERVE src0_sel:WORD_1\n"
    "v_mfma_f32_32x32x16_f16 v[36:51], v[132:135], v[112:115], v[36:51]\n"
    "s_waitcnt vmcnt(3)\n"
    "ds_read_b128 v[152:155], v241 offset:1392\n"
    "ds_write_b64 v246, v[164:165]\n"
    "ds_write_b64 v246, v[166:167] offset:336\n"
    "v_pk_fma_f16 v116, v120, s58, s58\n"
    "v_pk_fma_f16 v117, v121, s58, s58\n"
    "v_pk_fma_f16 v118, v122, s58, s58\n"
    "v_pk_fma_f16 v119, v123, s58, s58\n"
    "v_rcp_f16_e32 v120, v116\n"
    "v_mfma_f32_32x32x16_bf16 v[68:83], a[200:203], v[92:95], v[68:83]\n"
    "s_waitcnt vmcnt(2)\n"
    "ds_read_b128 v[124:127], v244 offset:49152\n"
    "ds_write_b64 v247, v[168:169]\n"
    "v_rcp_f16_e32 v121, v117\n"
    "v_rcp_f16_e32 v122, v118\n"
    "v_rcp_f16_e32 v123, v119\n"
    "v_rcp_f16_sdwa v120, v116 dst_sel:WORD_1 dst_unused:UNUSED_PRESERVE src0_sel:WORD_1\n"
    "v_mfma_f32_32x32x16_bf16 v[68:83], a[204:207], v[96:99], v[68:83]\n"
    "s_waitcnt lgkmcnt(9)\n"
    "ds_read_b128 v[128:131], v244 offset:50176\n"
    "ds_write_b64 v247, v[170:171] offset:336\n"
    "v_rcp_f16_sdwa v121, v117 dst_sel:WORD_1 dst_unused:UNUSED_PRESERVE src0_sel:WORD_1\n"
    "v_rcp_f16_sdwa v122, v118 dst_sel:WORD_1 dst_unused:UNUSED_PRESERVE src0_sel:WORD_1\n"
    "v_rcp_f16_sdwa v123, v119 dst_sel:WORD_1 dst_unused:UNUSED_PRESERVE src0_sel:WORD_1\n"
    "v_mfma_f32_16x16x32_bf16 v[156:159], v[224:227], v[136:139], 0\n"
    "ds_read_b128 v[132:135], v244 offset:51200\n"
    "v_pk_mul_f16 v108, v108, v120\n"
    "v_pk_mul_f16 v109, v109, v121\n"
    "v_pk_mul_f16 v110, v110, v122\n"
    "v_pk_mul_f16 v111, v111, v123\n"
    "v_cvt_pk_f16_f32 v112, v60, v61\n"
    "v_cvt_pk_f16_f32 v113, v62, v63\n"
    "v_cvt_pk_f16_f32 v114, v64, v65\n"
    "v_mfma_f32_32x32x16_bf16 v[68:83], a[208:211], v[100:103], v[68:83]\n"
    "s_waitcnt vmcnt(1) lgkmcnt(9)\n"
    "ds_write_b64 v248, v[172:173]\n"
    "v_cvt_pk_f16_f32 v115, v66, v67\n"
    "v_pk_fma_f16 v116, v112, v112, s57\n"
    "v_pk_fma_f16 v117, v113, v113, s57\n"
    "v_pk_fma_f16 v118, v114, v114, s57\n"
    "v_pk_fma_f16 v119, v115, v115, s57\n"
    "v_pk_mul_f16 v116, v116, v112\n"
    "v_pk_mul_f16 v117, v117, v113\n"
    "v_smfmac_f32_16x16x64_bf16 v[156:159], v[216:219], v[140:147], v240\n"
    "ds_write_b64 v248, v[174:175] offset:336\n"
    "v_pk_mul_f16 v118, v118, v114\n"
    "v_pk_mul_f16 v119, v119, v115\n"
    "v_exp_f16_e32 v120, v116\n"
    "v_exp_f16_e32 v121, v117\n"
    "v_mfma_f32_32x32x16_bf16 v[68:83], a[212:215], v[104:107], v[68:83]\n"
    "s_waitcnt vmcnt(0) lgkmcnt(6)\n"
    "ds_write_b64 v249, v[176:177]\n"
    "v_exp_f16_e32 v122, v118\n"
    "v_exp_f16_e32 v123, v119\n"
    "v_exp_f16_sdwa v120, v116 dst_sel:WORD_1 dst_unused:UNUSED_PRESERVE src0_sel:WORD_1\n"
    "v_mfma_f32_32x32x16_f16 v[4:19], v[124:127], v[108:111], v[4:19]\n"
    "ds_read_b128 v[124:127], v244 offset:52224\n"
    "v_exp_f16_sdwa v121, v117 dst_sel:WORD_1 dst_unused:UNUSED_PRESERVE src0_sel:WORD_1\n"
    "v_exp_f16_sdwa v122, v118 dst_sel:WORD_1 dst_unused:UNUSED_PRESERVE src0_sel:WORD_1\n"
    "v_exp_f16_sdwa v123, v119 dst_sel:WORD_1 dst_unused:UNUSED_PRESERVE src0_sel:WORD_1\n"
    "v_smfmac_f32_16x16x64_bf16 v[156:159], v[220:223], v[148:155], v240\n"
    "s_waitcnt lgkmcnt(6)\n"
    "ds_write_b64 v249, v[178:179] offset:336\n"
    "s_add_u32 s70, s54, 0x4000\n"
    "s_cmp_ge_u32 s70, 0xc000\n"
    "s_cselect_b32 s53, s76, s77\n"
    "s_add_u32 s54, s54, s53\n"
    "v_add_u32 v246, s53, v246\n"
    "v_pk_fma_f16 v116, v120, s58, s58\n"
    "v_add_u32 v247, s53, v247\n"
    "v_add_u32 v248, s53, v248\n"
    "v_add_u32 v249, s53, v249\n"
    "v_pk_fma_f16 v117, v121, s58, s58\n"
    "v_pk_fma_f16 v118, v122, s58, s58\n"
    "v_pk_fma_f16 v119, v123, s58, s58\n"
    "v_rcp_f16_e32 v120, v116\n"
    "v_rcp_f16_e32 v121, v117\n"
    "v_mfma_f32_32x32x16_f16 v[20:35], v[128:131], v[108:111], v[20:35]\n"
    "s_waitcnt lgkmcnt(5)\n"
    "ds_read_b128 v[128:131], v244 offset:53248\n"
    "buffer_load_dwordx4 v[164:167], v253, s[36:39], s56 offen\n"
    "v_cvt_pk_bf16_f32 v160, v156, v157\n"
    "v_cvt_pk_bf16_f32 v161, v158, v159\n"
    "v_rcp_f16_e32 v122, v118\n"
    "v_rcp_f16_e32 v123, v119\n"
    "v_rcp_f16_sdwa v120, v116 dst_sel:WORD_1 dst_unused:UNUSED_PRESERVE src0_sel:WORD_1\n"
    "v_rcp_f16_sdwa v121, v117 dst_sel:WORD_1 dst_unused:UNUSED_PRESERVE src0_sel:WORD_1\n"
    "v_rcp_f16_sdwa v122, v118 dst_sel:WORD_1 dst_unused:UNUSED_PRESERVE src0_sel:WORD_1\n"
    "v_rcp_f16_sdwa v123, v119 dst_sel:WORD_1 dst_unused:UNUSED_PRESERVE src0_sel:WORD_1\n"
    "v_mfma_f32_32x32x16_f16 v[36:51], v[132:135], v[108:111], v[36:51]\n"
    "s_waitcnt lgkmcnt(2)\n"
    "ds_read_b128 v[132:135], v244 offset:54272\n"
    "ds_write_b64 v242, v[160:161] offset:13344\n"
    "v_pk_mul_f16 v112, v112, v120\n"
    "v_pk_mul_f16 v113, v113, v121\n"
    "v_pk_mul_f16 v114, v114, v122\n"
    "v_pk_mul_f16 v115, v115, v123\n"
    "v_cvt_pk_f16_f32 v108, v68, v69\n"
    "v_cvt_pk_f16_f32 v109, v70, v71\n"
    "v_mfma_f32_32x32x16_f16 v[4:19], v[124:127], v[112:115], v[4:19]\n"
    "ds_read_b128 v[136:139], v241 offset:1744\n"
    "v_cvt_pk_f16_f32 v110, v72, v73\n"
    "v_cvt_pk_f16_f32 v111, v74, v75\n"
    "v_pk_fma_f16 v116, v108, v108, s57\n"
    "v_pk_fma_f16 v117, v109, v109, s57\n"
    "v_pk_fma_f16 v118, v110, v110, s57\n"
    "v_mfma_f32_32x32x16_bf16 v[52:67], a[216:219], v[84:87], 0\n"
    "s_waitcnt lgkmcnt(3)\n"
    "ds_read_b128 v[140:143], v241 offset:1680\n"
    "v_pk_fma_f16 v119, v111, v111, s57\n"
    "v_pk_mul_f16 v116, v116, v108\n"
    "v_pk_mul_f16 v117, v117, v109\n"
    "v_pk_mul_f16 v118, v118, v110\n"
    "v_pk_mul_f16 v119, v119, v111\n"
    "v_exp_f16_e32 v120, v116\n"
    "v_mfma_f32_32x32x16_f16 v[20:35], v[128:131], v[112:115], v[20:35]\n"
    "ds_read_b128 v[144:147], v241 offset:1696\n"
    "v_exp_f16_e32 v121, v117\n"
    "v_exp_f16_e32 v122, v118\n"
    "v_exp_f16_e32 v123, v119\n"
    "v_exp_f16_sdwa v120, v116 dst_sel:WORD_1 dst_unused:UNUSED_PRESERVE src0_sel:WORD_1\n"
    "v_mfma_f32_32x32x16_bf16 v[52:67], a[220:223], v[88:91], v[52:67]\n"
    "s_waitcnt lgkmcnt(4)\n"
    "ds_read_b128 v[148:151], v241 offset:1712\n"
    "buffer_load_dwordx4 v[168:171], v253, s[36:39], s56 offen offset:32\n"
    "v_exp_f16_sdwa v121, v117 dst_sel:WORD_1 dst_unused:UNUSED_PRESERVE src0_sel:WORD_1\n"
    "v_exp_f16_sdwa v122, v118 dst_sel:WORD_1 dst_unused:UNUSED_PRESERVE src0_sel:WORD_1\n"
    "v_exp_f16_sdwa v123, v119 dst_sel:WORD_1 dst_unused:UNUSED_PRESERVE src0_sel:WORD_1\n"
    "v_mfma_f32_32x32x16_f16 v[36:51], v[132:135], v[112:115], v[36:51]\n"
    "s_waitcnt lgkmcnt(3)\n"
    "ds_read_b128 v[152:155], v241 offset:1728\n"
    "v_pk_fma_f16 v116, v120, s58, s58\n"
    "v_pk_fma_f16 v117, v121, s58, s58\n"
    "v_pk_fma_f16 v118, v122, s58, s58\n"
    "v_pk_fma_f16 v119, v123, s58, s58\n"
    "v_rcp_f16_e32 v120, v116\n"
    "v_mfma_f32_16x16x32_bf16 v[156:159], v[236:239], v[136:139], 0\n"
    "v_rcp_f16_e32 v121, v117\n"
    "v_rcp_f16_e32 v122, v118\n"
    "v_rcp_f16_e32 v123, v119\n"
    "v_rcp_f16_sdwa v120, v116 dst_sel:WORD_1 dst_unused:UNUSED_PRESERVE src0_sel:WORD_1\n"
    "v_mfma_f32_32x32x16_bf16 v[52:67], a[224:227], v[92:95], v[52:67]\n"
    "s_waitcnt lgkmcnt(2)\n"
    "ds_read_b128 v[124:127], v244 offset:55296\n"
    "v_rcp_f16_sdwa v121, v117 dst_sel:WORD_1 dst_unused:UNUSED_PRESERVE src0_sel:WORD_1\n"
    "v_rcp_f16_sdwa v122, v118 dst_sel:WORD_1 dst_unused:UNUSED_PRESERVE src0_sel:WORD_1\n"
    "v_rcp_f16_sdwa v123, v119 dst_sel:WORD_1 dst_unused:UNUSED_PRESERVE src0_sel:WORD_1\n"
    "v_smfmac_f32_16x16x64_bf16 v[156:159], v[228:231], v[140:147], v240\n"
    "ds_read_b128 v[128:131], v244 offset:56320\n"
    "v_pk_mul_f16 v108, v108, v120\n"
    "v_pk_mul_f16 v109, v109, v121\n"
    "v_pk_mul_f16 v110, v110, v122\n"
    "v_pk_mul_f16 v111, v111, v123\n"
    "v_cvt_pk_f16_f32 v112, v76, v77\n"
    "v_cvt_pk_f16_f32 v113, v78, v79\n"
    "v_cvt_pk_f16_f32 v114, v80, v81\n"
    "v_mfma_f32_32x32x16_bf16 v[52:67], a[228:231], v[96:99], v[52:67]\n"
    "s_waitcnt lgkmcnt(2)\n"
    "ds_read_b128 v[132:135], v244 offset:57344\n"
    "buffer_load_dwordx4 v[172:175], v253, s[36:39], s56 offen offset:64\n"
    "v_cvt_pk_f16_f32 v115, v82, v83\n"
    "v_pk_fma_f16 v116, v112, v112, s57\n"
    "v_pk_fma_f16 v117, v113, v113, s57\n"
    "v_pk_fma_f16 v118, v114, v114, s57\n"
    "v_pk_fma_f16 v119, v115, v115, s57\n"
    "v_pk_mul_f16 v116, v116, v112\n"
    "v_pk_mul_f16 v117, v117, v113\n"
    "v_smfmac_f32_16x16x64_bf16 v[156:159], v[232:235], v[148:155], v240\n"
    "v_pk_mul_f16 v118, v118, v114\n"
    "v_pk_mul_f16 v119, v119, v115\n"
    "v_exp_f16_e32 v120, v116\n"
    "v_exp_f16_e32 v121, v117\n"
    "v_mfma_f32_32x32x16_bf16 v[52:67], a[232:235], v[100:103], v[52:67]\n"
    "s_waitcnt lgkmcnt(2)\n"
    "buffer_load_dwordx4 v[176:179], v253, s[36:39], s56 offen offset:96\n"
    "v_exp_f16_e32 v122, v118\n"
    "v_cvt_pk_bf16_f32 v162, v156, v157\n"
    "v_cvt_pk_bf16_f32 v163, v158, v159\n"
    "v_exp_f16_e32 v123, v119\n"
    "v_exp_f16_sdwa v120, v116 dst_sel:WORD_1 dst_unused:UNUSED_PRESERVE src0_sel:WORD_1\n"
    "v_mfma_f32_32x32x16_f16 v[4:19], v[124:127], v[108:111], v[4:19]\n"
    "ds_read_b128 v[124:127], v244 offset:58368\n"
    "ds_write_b64 v242, v[162:163] offset:13352\n"
    "v_exp_f16_sdwa v121, v117 dst_sel:WORD_1 dst_unused:UNUSED_PRESERVE src0_sel:WORD_1\n"
    "v_exp_f16_sdwa v122, v118 dst_sel:WORD_1 dst_unused:UNUSED_PRESERVE src0_sel:WORD_1\n"
    "v_exp_f16_sdwa v123, v119 dst_sel:WORD_1 dst_unused:UNUSED_PRESERVE src0_sel:WORD_1\n"
    "v_mfma_f32_32x32x16_bf16 v[52:67], a[236:239], v[104:107], v[52:67]\n"
    "s_waitcnt lgkmcnt(3)\n"
    "ds_read_b128 v[140:143], v245 offset:8192\n"
    "v_pk_fma_f16 v116, v120, s58, s58\n"
    "v_pk_fma_f16 v117, v121, s58, s58\n"
    "v_pk_fma_f16 v118, v122, s58, s58\n"
    "v_pk_fma_f16 v119, v123, s58, s58\n"
    "v_rcp_f16_e32 v120, v116\n"
    "v_rcp_f16_e32 v121, v117\n"
    "v_rcp_f16_e32 v122, v118\n"
    "v_mfma_f32_32x32x16_f16 v[20:35], v[128:131], v[108:111], v[20:35]\n"
    "s_waitcnt lgkmcnt(3)\n"
    "ds_read_b128 v[128:131], v244 offset:59392\n"
    "ds_read_b128 v[144:147], v245 offset:9216\n"
    "v_rcp_f16_e32 v123, v119\n"
    "v_rcp_f16_sdwa v120, v116 dst_sel:WORD_1 dst_unused:UNUSED_PRESERVE src0_sel:WORD_1\n"
    "v_rcp_f16_sdwa v121, v117 dst_sel:WORD_1 dst_unused:UNUSED_PRESERVE src0_sel:WORD_1\n"
    "v_add_u32 v241, 0x4000, v241\n"
    "v_add_u32 v254, 0xffff4000, v241\n"
    "v_min_u32 v241, v241, v254\n"
    "ds_read_b128 v[148:151], v245 offset:10240\n"
    "ds_read_b128 v[152:155], v245 offset:11264\n"
    "v_rcp_f16_sdwa v122, v118 dst_sel:WORD_1 dst_unused:UNUSED_PRESERVE src0_sel:WORD_1\n"
    "v_rcp_f16_sdwa v123, v119 dst_sel:WORD_1 dst_unused:UNUSED_PRESERVE src0_sel:WORD_1\n"
    "v_mfma_f32_32x32x16_f16 v[36:51], v[132:135], v[108:111], v[36:51]\n"
    "ds_read_b128 v[132:135], v244 offset:60416\n"
    "v_pk_mul_f16 v112, v112, v120\n"
    "v_pk_mul_f16 v113, v113, v121\n"
    "v_pk_mul_f16 v114, v114, v122\n"
    "v_pk_mul_f16 v115, v115, v123\n"
    "s_waitcnt lgkmcnt(6)\n"
    "s_barrier\n"
    "v_cvt_pk_f16_f32 v108, v52, v53\n"
    "v_cvt_pk_f16_f32 v109, v54, v55\n"
    "v_mfma_f32_32x32x16_f16 v[4:19], v[124:127], v[112:115], v[4:19]\n"
    "s_waitcnt lgkmcnt(5)\n"
    "ds_read_b128 v[124:127], v244 offset:61440\n"
    "v_cvt_pk_f16_f32 v110, v56, v57\n"
    "v_cvt_pk_f16_f32 v111, v58, v59\n"
    "v_pk_fma_f16 v116, v108, v108, s57\n"
    "v_pk_fma_f16 v117, v109, v109, s57\n"
    "v_pk_fma_f16 v118, v110, v110, s57\n"
    "v_pk_fma_f16 v119, v111, v111, s57\n"
    "v_mfma_f32_32x32x16_bf16 v[68:83], v[140:143], v[84:87], 0\n"
    "s_waitcnt lgkmcnt(5)\n"
    "ds_read_b128 v[140:143], v245 offset:12288\n"
    "ds_read_b128 v[84:87], v243\n"
    "v_pk_mul_f16 v116, v116, v108\n"
    "v_pk_mul_f16 v117, v117, v109\n"
    "v_pk_mul_f16 v118, v118, v110\n"
    "v_pk_mul_f16 v119, v119, v111\n"
    "v_exp_f16_e32 v120, v116\n"
    "v_exp_f16_e32 v121, v117\n"
    "v_mfma_f32_32x32x16_f16 v[20:35], v[128:131], v[112:115], v[20:35]\n"
    "s_waitcnt lgkmcnt(6)\n"
    "ds_read_b128 v[128:131], v244 offset:62464\n"
    "buffer_load_dwordx4 v[160:163], v253, s[36:39], s56 offen offset:160\n"
    "v_exp_f16_e32 v122, v118\n"
    "v_exp_f16_e32 v123, v119\n"
    "v_exp_f16_sdwa v120, v116 dst_sel:WORD_1 dst_unused:UNUSED_PRESERVE src0_sel:WORD_1\n"
    "v_exp_f16_sdwa v121, v117 dst_sel:WORD_1 dst_unused:UNUSED_PRESERVE src0_sel:WORD_1\n"
    "v_mfma_f32_32x32x16_bf16 v[68:83], v[144:147], v[88:91], v[68:83]\n"
    "s_waitcnt lgkmcnt(4)\n"
    "ds_read_b128 v[144:147], v245 offset:13312\n"
    "ds_read_b128 v[88:91], v243 offset:32\n"
    "v_exp_f16_sdwa v122, v118 dst_sel:WORD_1 dst_unused:UNUSED_PRESERVE src0_sel:WORD_1\n"
    "v_exp_f16_sdwa v123, v119 dst_sel:WORD_1 dst_unused:UNUSED_PRESERVE src0_sel:WORD_1\n"
    "v_pk_fma_f16 v116, v120, s58, s58\n"
    "v_pk_fma_f16 v117, v121, s58, s58\n"
    "v_pk_fma_f16 v118, v122, s58, s58\n"
    "v_pk_fma_f16 v119, v123, s58, s58\n"
    "v_mfma_f32_32x32x16_f16 v[36:51], v[132:135], v[112:115], v[36:51]\n"
    "ds_read_b128 v[132:135], v244 offset:63488\n"
    "v_rcp_f16_e32 v120, v116\n"
    "v_rcp_f16_e32 v121, v117\n"
    "v_rcp_f16_e32 v122, v118\n"
    "v_rcp_f16_e32 v123, v119\n"
    "v_mfma_f32_32x32x16_bf16 v[68:83], v[148:151], v[92:95], v[68:83]\n"
    "ds_read_b128 v[92:95], v243 offset:64\n"
    "v_rcp_f16_sdwa v120, v116 dst_sel:WORD_1 dst_unused:UNUSED_PRESERVE src0_sel:WORD_1\n"
    "v_rcp_f16_sdwa v121, v117 dst_sel:WORD_1 dst_unused:UNUSED_PRESERVE src0_sel:WORD_1\n"
    "v_rcp_f16_sdwa v122, v118 dst_sel:WORD_1 dst_unused:UNUSED_PRESERVE src0_sel:WORD_1\n"
    "v_rcp_f16_sdwa v123, v119 dst_sel:WORD_1 dst_unused:UNUSED_PRESERVE src0_sel:WORD_1\n"
    "v_mfma_f32_32x32x16_bf16 v[68:83], v[152:155], v[96:99], v[68:83]\n"
    "s_waitcnt lgkmcnt(6)\n"
    "ds_read_b128 v[96:99], v243 offset:96\n"
    "v_pk_mul_f16 v108, v108, v120\n"
    "v_pk_mul_f16 v109, v109, v121\n"
    "v_pk_mul_f16 v110, v110, v122\n"
    "v_pk_mul_f16 v111, v111, v123\n"
    "v_cvt_pk_f16_f32 v112, v60, v61\n"
    "v_cvt_pk_f16_f32 v113, v62, v63\n"
    "v_cvt_pk_f16_f32 v114, v64, v65\n"
    "v_mfma_f32_32x32x16_bf16 v[68:83], v[140:143], v[100:103], v[68:83]\n"
    "s_waitcnt lgkmcnt(4)\n"
    "ds_read_b128 v[100:103], v243 offset:128\n"
    "v_cvt_pk_f16_f32 v115, v66, v67\n"
    "v_pk_fma_f16 v116, v112, v112, s57\n"
    "v_mov_b32 v156, 0\n"
    "v_pk_fma_f16 v117, v113, v113, s57\n"
    "v_mov_b32 v157, 0\n"
    "v_mov_b32 v158, 0\n"
    "v_pk_fma_f16 v118, v114, v114, s57\n"
    "v_mov_b32 v159, 0\n"
    "v_pk_fma_f16 v119, v115, v115, s57\n"
    "v_pk_mul_f16 v116, v116, v112\n"
    "v_pk_mul_f16 v117, v117, v113\n"
    "v_pk_mul_f16 v118, v118, v114\n"
    "v_mfma_f32_32x32x16_bf16 v[68:83], v[144:147], v[104:107], v[68:83]\n"
    "s_waitcnt lgkmcnt(2)\n"
    "ds_read_b128 v[104:107], v243 offset:160\n"
    "v_pk_mul_f16 v119, v119, v115\n"
    "v_exp_f16_e32 v120, v116\n"
    "v_exp_f16_e32 v121, v117\n"
    "v_exp_f16_e32 v122, v118\n"
    "v_dot2c_f32_bf16 v156, v84, v84\n"
    "v_dot2c_f32_bf16 v158, 0x3f803f80, v84\n"
    "v_dot2c_f32_bf16 v157, v85, v85\n"
    "v_dot2c_f32_bf16 v159, 0x3f803f80, v85\n"
    "v_dot2c_f32_bf16 v156, v86, v86\n"
    "v_dot2c_f32_bf16 v158, 0x3f803f80, v86\n"
    "v_dot2c_f32_bf16 v157, v87, v87\n"
    "v_dot2c_f32_bf16 v159, 0x3f803f80, v87\n"
    "v_dot2c_f32_bf16 v156, v88, v88\n"
    "v_dot2c_f32_bf16 v158, 0x3f803f80, v88\n"
    "v_dot2c_f32_bf16 v157, v89, v89\n"
    "v_dot2c_f32_bf16 v159, 0x3f803f80, v89\n"
    "v_dot2c_f32_bf16 v156, v90, v90\n"
    "v_dot2c_f32_bf16 v158, 0x3f803f80, v90\n"
    "v_dot2c_f32_bf16 v157, v91, v91\n"
    "v_dot2c_f32_bf16 v159, 0x3f803f80, v91\n"
    "v_dot2c_f32_bf16 v156, v92, v92\n"
    "v_dot2c_f32_bf16 v158, 0x3f803f80, v92\n"
    "v_dot2c_f32_bf16 v157, v93, v93\n"
    "v_dot2c_f32_bf16 v159, 0x3f803f80, v93\n"
    "v_dot2c_f32_bf16 v156, v94, v94\n"
    "v_dot2c_f32_bf16 v158, 0x3f803f80, v94\n"
    "v_dot2c_f32_bf16 v157, v95, v95\n"
    "v_dot2c_f32_bf16 v159, 0x3f803f80, v95\n"
    "v_exp_f16_e32 v123, v119\n"
    "v_exp_f16_sdwa v120, v116 dst_sel:WORD_1 dst_unused:UNUSED_PRESERVE src0_sel:WORD_1\n"
    "v_mfma_f32_32x32x16_f16 v[4:19], v[124:127], v[108:111], v[4:19]\n"
    "ds_read_b128 v[124:127], v244 offset:64512\n"
    "v_exp_f16_sdwa v121, v117 dst_sel:WORD_1 dst_unused:UNUSED_PRESERVE src0_sel:WORD_1\n"
    "v_exp_f16_sdwa v122, v118 dst_sel:WORD_1 dst_unused:UNUSED_PRESERVE src0_sel:WORD_1\n"
    "v_exp_f16_sdwa v123, v119 dst_sel:WORD_1 dst_unused:UNUSED_PRESERVE src0_sel:WORD_1\n"
    "v_pk_fma_f16 v116, v120, s58, s58\n"
    "v_pk_fma_f16 v117, v121, s58, s58\n"
    "v_pk_fma_f16 v118, v122, s58, s58\n"
    "v_pk_fma_f16 v119, v123, s58, s58\n"
    "v_mfma_f32_32x32x16_f16 v[20:35], v[128:131], v[108:111], v[20:35]\n"
    "s_waitcnt lgkmcnt(1)\n"
    "ds_read_b128 v[128:131], v245\n"
    "v_rcp_f16_e32 v120, v116\n"
    "v_rcp_f16_e32 v121, v117\n"
    "v_rcp_f16_e32 v122, v118\n"
    "v_rcp_f16_e32 v123, v119\n"
    "v_dot2c_f32_bf16 v156, v96, v96\n"
    "v_dot2c_f32_bf16 v158, 0x3f803f80, v96\n"
    "v_dot2c_f32_bf16 v157, v97, v97\n"
    "v_dot2c_f32_bf16 v159, 0x3f803f80, v97\n"
    "v_dot2c_f32_bf16 v156, v98, v98\n"
    "v_dot2c_f32_bf16 v158, 0x3f803f80, v98\n"
    "v_dot2c_f32_bf16 v157, v99, v99\n"
    "v_dot2c_f32_bf16 v159, 0x3f803f80, v99\n"
    "v_dot2c_f32_bf16 v156, v100, v100\n"
    "v_dot2c_f32_bf16 v158, 0x3f803f80, v100\n"
    "v_dot2c_f32_bf16 v157, v101, v101\n"
    "v_dot2c_f32_bf16 v159, 0x3f803f80, v101\n"
    "v_dot2c_f32_bf16 v156, v102, v102\n"
    "v_dot2c_f32_bf16 v158, 0x3f803f80, v102\n"
    "v_dot2c_f32_bf16 v157, v103, v103\n"
    "v_dot2c_f32_bf16 v159, 0x3f803f80, v103\n"
    "v_dot2c_f32_bf16 v156, v104, v104\n"
    "v_dot2c_f32_bf16 v158, 0x3f803f80, v104\n"
    "v_dot2c_f32_bf16 v157, v105, v105\n"
    "v_dot2c_f32_bf16 v159, 0x3f803f80, v105\n"
    "v_dot2c_f32_bf16 v156, v106, v106\n"
    "v_dot2c_f32_bf16 v158, 0x3f803f80, v106\n"
    "v_dot2c_f32_bf16 v157, v107, v107\n"
    "v_dot2c_f32_bf16 v159, 0x3f803f80, v107\n"
    "v_rcp_f16_sdwa v120, v116 dst_sel:WORD_1 dst_unused:UNUSED_PRESERVE src0_sel:WORD_1\n"
    "v_rcp_f16_sdwa v121, v117 dst_sel:WORD_1 dst_unused:UNUSED_PRESERVE src0_sel:WORD_1\n"
    "v_mfma_f32_32x32x16_f16 v[36:51], v[132:135], v[108:111], v[36:51]\n"
    "s_waitcnt lgkmcnt(1)\n"
    "ds_read_b128 v[132:135], v245 offset:1024\n"
    "v_rcp_f16_sdwa v122, v118 dst_sel:WORD_1 dst_unused:UNUSED_PRESERVE src0_sel:WORD_1\n"
    "v_rcp_f16_sdwa v123, v119 dst_sel:WORD_1 dst_unused:UNUSED_PRESERVE src0_sel:WORD_1\n"
    "v_pk_mul_f16 v112, v112, v120\n"
    "v_pk_mul_f16 v113, v113, v121\n"
    "v_add_f32 v156, v156, v157\n"
    "v_add_f32 v158, v158, v159\n"
    "v_pk_mul_f16 v114, v114, v122\n"
    "v_mov_b32 v157, v156\n"
    "v_pk_mul_f16 v115, v115, v123\n"
    "v_mov_b32 v159, v158\n"
    "v_permlane32_swap_b32 v156, v157\n"
    "s_nop 0\n"
    "v_permlane32_swap_b32 v158, v159\n"
    "v_cvt_pk_f16_f32 v108, v68, v69\n"
    "v_add_f32 v158, v158, v159\n"
    "v_add_f32 v156, v156, v157\n"
    "v_cvt_pk_f16_f32 v109, v70, v71\n"
    "v_mfma_f32_32x32x16_f16 v[4:19], v[124:127], v[112:115], v[4:19]\n"
    "s_waitcnt lgkmcnt(1)\n"
    "ds_read_b128 v[124:127], v245 offset:2048\n"
    "ds_read_b128 v[140:143], v245 offset:14336\n"
    "s_mov_b32 s55, s56\n"
    "s_add_u32 s78, s56, 98304\n"
    "s_add_u32 s56, s56, 786432\n"
    "v_mul_f32 v254, 0x3c2aaaab, v158\n"
    "v_cvt_pk_f16_f32 v110, v72, v73\n"
    "v_mul_f32 v255, v254, v254\n"
    "v_cvt_pk_f16_f32 v111, v74, v75\n"
    "v_fma_f32 v255, v156, s79, -v255\n"
    "v_max_f32 v255, 0, v255\n"
    "v_pk_fma_f16 v116, v108, v108, s57\n"
    "v_add_f32 v255, 0x3727c5ac, v255\n"
    "v_pk_fma_f16 v117, v109, v109, s57\n"
    "v_rsq_f32 v255, v255\n"
    "v_pk_fma_f16 v118, v110, v110, s57\n"
    "v_mul_f32 v255, 0xbeeff558, v255\n"
    "v_pk_fma_f16 v119, v111, v111, s57\n"
    "v_mul_f32 v254, v254, v255\n"
    "v_pk_mul_f16 v116, v116, v108\n"
    "v_pk_mul_f16 v117, v117, v109\n"
    "v_pk_mul_f16 v118, v118, v110\n"
    "v_mfma_f32_32x32x16_f16 v[20:35], v[128:131], v[112:115], v[20:35]\n"
    "s_waitcnt lgkmcnt(2)\n"
    "ds_read_b128 v[128:131], v245 offset:3072\n"
    "ds_read_b128 v[144:147], v245 offset:15360\n"
    "ds_read_b128 v[148:151], v245 offset:16384\n"
    "v_pk_mul_f16 v119, v119, v111\n"
    "v_lshlrev_b32 v0, 16, v84\n"
    "v_exp_f16_e32 v120, v116\n"
    "v_and_b32 v3, s59, v84\n"
    "v_fma_f32 v0, v0, v255, -v254\n"
    "v_exp_f16_e32 v121, v117\n"
    "v_fma_f32 v3, v3, v255, -v254\n"
    "v_cvt_pk_bf16_f32 v84, v0, v3\n"
    "v_exp_f16_e32 v122, v118\n"
    "v_lshlrev_b32 v0, 16, v85\n"
    "v_and_b32 v3, s59, v85\n"
    "v_exp_f16_e32 v123, v119\n"
    "v_fma_f32 v0, v0, v255, -v254\n"
    "v_fma_f32 v3, v3, v255, -v254\n"
    "v_mfma_f32_32x32x16_f16 v[36:51], v[132:135], v[112:115], v[36:51]\n"
    "s_waitcnt lgkmcnt(3)\n"
    "ds_read_b128 v[132:135], v245 offset:4096\n"
    "ds_read_b128 v[152:155], v245 offset:17408\n"
    "ds_read_b128 v[136:139], v245 offset:20480\n"
    "v_exp_f16_sdwa v120, v116 dst_sel:WORD_1 dst_unused:UNUSED_PRESERVE src0_sel:WORD_1\n"
    "v_cvt_pk_bf16_f32 v85, v0, v3\n"
    "v_lshlrev_b32 v0, 16, v86\n"
    "v_exp_f16_sdwa v121, v117 dst_sel:WORD_1 dst_unused:UNUSED_PRESERVE src0_sel:WORD_1\n"
    "v_and_b32 v3, s59, v86\n"
    "v_fma_f32 v0, v0, v255, -v254\n"
    "v_exp_f16_sdwa v122, v118 dst_sel:WORD_1 dst_unused:UNUSED_PRESERVE src0_sel:WORD_1\n"
    "v_fma_f32 v3, v3, v255, -v254\n"
    "v_cvt_pk_bf16_f32 v86, v0, v3\n"
    "v_lshlrev_b32 v0, 16, v87\n"
    "v_exp_f16_sdwa v123, v119 dst_sel:WORD_1 dst_unused:UNUSED_PRESERVE src0_sel:WORD_1\n"
    "v_and_b32 v3, s59, v87\n"
    "v_fma_f32 v0, v0, v255, -v254\n"
    "v_pk_fma_f16 v116, v120, s58, s58\n"
    "v_fma_f32 v3, v3, v255, -v254\n"
    "v_pk_fma_f16 v117, v121, s58, s58\n"
    "v_cvt_pk_bf16_f32 v87, v0, v3\n"
    "v_pk_fma_f16 v118, v122, s58, s58\n"
    "v_lshlrev_b32 v0, 16, v88\n"
    "v_pk_fma_f16 v119, v123, s58, s58\n"
    "v_mfma_f32_32x32x16_bf16 v[52:67], v[140:143], v[84:87], 0\n"
    "s_waitcnt vmcnt(4) lgkmcnt(0)\n"
    "ds_read_b128 v[140:143], v245 offset:18432\n"
    "v_and_b32 v3, s59, v88\n"
    "v_rcp_f16_e32 v120, v116\n"
    "v_fma_f32 v0, v0, v255, -v254\n"
    "v_fma_f32 v3, v3, v255, -v254\n"
    "v_rcp_f16_e32 v121, v117\n"
    "v_cvt_pk_bf16_f32 v88, v0, v3\n"
    "v_lshlrev_b32 v0, 16, v89\n"
    "v_rcp_f16_e32 v122, v118\n"
    "v_and_b32 v3, s59, v89\n"
    "v_fma_f32 v0, v0, v255, -v254\n"
    "v_rcp_f16_e32 v123, v119\n"
    "v_fma_f32 v3, v3, v255, -v254\n"
    "v_mfma_f32_32x32x16_bf16 v[4:19], v[136:139], v[164:167], v[4:19]\n"
    "s_waitcnt vmcnt(2)\n"
    "v_cvt_pk_bf16_f32 v89, v0, v3\n"
    "v_lshlrev_b32 v0, 16, v90\n"
    "v_rcp_f16_sdwa v120, v116 dst_sel:WORD_1 dst_unused:UNUSED_PRESERVE src0_sel:WORD_1\n"
    "v_and_b32 v3, s59, v90\n"
    "v_fma_f32 v0, v0, v255, -v254\n"
    "v_rcp_f16_sdwa v121, v117 dst_sel:WORD_1 dst_unused:UNUSED_PRESERVE src0_sel:WORD_1\n"
    "v_fma_f32 v3, v3, v255, -v254\n"
    "v_cvt_pk_bf16_f32 v90, v0, v3\n"
    "v_rcp_f16_sdwa v122, v118 dst_sel:WORD_1 dst_unused:UNUSED_PRESERVE src0_sel:WORD_1\n"
    "v_lshlrev_b32 v0, 16, v91\n"
    "v_mfma_f32_32x32x16_bf16 v[20:35], v[136:139], v[172:175], v[20:35]\n"
    "v_and_b32 v3, s59, v91\n"
    "v_rcp_f16_sdwa v123, v119 dst_sel:WORD_1 dst_unused:UNUSED_PRESERVE src0_sel:WORD_1\n"
    "v_fma_f32 v0, v0, v255, -v254\n"
    "v_fma_f32 v3, v3, v255, -v254\n"
    "v_pk_mul_f16 v108, v108, v120\n"
    "v_cvt_pk_bf16_f32 v91, v0, v3\n"
    "v_pk_mul_f16 v109, v109, v121\n"
    "v_lshlrev_b32 v0, 16, v92\n"
    "v_pk_mul_f16 v110, v110, v122\n"
    "v_mfma_f32_32x32x16_bf16 v[52:67], v[144:147], v[88:91], v[52:67]\n"
    "ds_read_b128 v[144:147], v245 offset:19456\n"
    "v_and_b32 v3, s59, v92\n"
    "v_pk_mul_f16 v111, v111, v123\n"
    "v_fma_f32 v0, v0, v255, -v254\n"
    "v_cvt_pk_f16_f32 v112, v76, v77\n"
    "v_fma_f32 v3, v3, v255, -v254\n"
    "v_cvt_pk_f16_f32 v113, v78, v79\n"
    "v_cvt_pk_bf16_f32 v92, v0, v3\n"
    "v_lshlrev_b32 v0, 16, v93\n"
    "v_cvt_pk_f16_f32 v114, v80, v81\n"
    "v_and_b32 v3, s59, v93\n"
    "v_cvt_pk_f16_f32 v115, v82, v83\n"
    "v_fma_f32 v0, v0, v255, -v254\n"
    "v_pk_fma_f16 v116, v112, v112, s57\n"
    "v_fma_f32 v3, v3, v255, -v254\n"
    "v_pk_fma_f16 v117, v113, v113, s57\n"
    "v_mfma_f32_32x32x16_f16 v[4:19], v[124:127], v[108:111], v[4:19]\n"
    "ds_read_b128 v[124:127], v245 offset:5120\n"
    "ds_read_b128 v[156:159], v245 offset:21504\n"
    "v_cvt_pk_bf16_f32 v93, v0, v3\n"
    "v_pk_fma_f16 v118, v114, v114, s57\n"
    "v_lshlrev_b32 v0, 16, v94\n"
    "v_pk_fma_f16 v119, v115, v115, s57\n"
    "v_and_b32 v3, s59, v94\n"
    "v_pk_mul_f16 v116, v116, v112\n"
    "v_fma_f32 v0, v0, v255, -v254\n"
    "v_pk_mul_f16 v117, v117, v113\n"
    "v_fma_f32 v3, v3, v255, -v254\n"
    "v_pk_mul_f16 v118, v118, v114\n"
    "v_cvt_pk_bf16_f32 v94, v0, v3\n"
    "v_pk_mul_f16 v119, v119, v115\n"
    "v_lshlrev_b32 v0, 16, v95\n"
    "v_exp_f16_e32 v120, v116\n"
    "v_and_b32 v3, s59, v95\n"
    "v_fma_f32 v0, v0, v255, -v254\n"
    "v_mfma_f32_32x32x16_f16 v[20:35], v[128:131], v[108:111], v[20:35]\n"
    "ds_read_b128 v[128:131], v245 offset:6144\n"
    "v_exp_f16_e32 v121, v117\n"
    "v_fma_f32 v3, v3, v255, -v254\n"
    "v_cvt_pk_bf16_f32 v95, v0, v3\n"
    "v_lshlrev_b32 v0, 16, v96\n"
    "v_exp_f16_e32 v122, v118\n"
    "v_mfma_f32_32x32x16_bf16 v[52:67], v[148:151], v[92:95], v[52:67]\n"
    "v_and_b32 v3, s59, v96\n"
    "v_fma_f32 v0, v0, v255, -v254\n"
    "v_exp_f16_e32 v123, v119\n"
    "v_fma_f32 v3, v3, v255, -v254\n"
    "v_cvt_pk_bf16_f32 v96, v0, v3\n"
    "v_exp_f16_sdwa v120, v116 dst_sel:WORD_1 dst_unused:UNUSED_PRESERVE src0_sel:WORD_1\n"
    "v_lshlrev_b32 v0, 16, v97\n"
    "v_mfma_f32_32x32x16_f16 v[36:51], v[132:135], v[108:111], v[36:51]\n"
    "ds_read_b128 v[132:135], v245 offset:7168\n"
    "v_and_b32 v3, s59, v97\n"
    "v_exp_f16_sdwa v121, v117 dst_sel:WORD_1 dst_unused:UNUSED_PRESERVE src0_sel:WORD_1\n"
    "v_fma_f32 v0, v0, v255, -v254\n"
    "v_fma_f32 v3, v3, v255, -v254\n"
    "v_exp_f16_sdwa v122, v118 dst_sel:WORD_1 dst_unused:UNUSED_PRESERVE src0_sel:WORD_1\n"
    "v_cvt_pk_bf16_f32 v97, v0, v3\n"
    "v_lshlrev_b32 v0, 16, v98\n"
    "v_exp_f16_sdwa v123, v119 dst_sel:WORD_1 dst_unused:UNUSED_PRESERVE src0_sel:WORD_1\n"
    "v_and_b32 v3, s59, v98\n"
    "v_fma_f32 v0, v0, v255, -v254\n"
    "v_pk_fma_f16 v116, v120, s58, s58\n"
    "v_mfma_f32_32x32x16_bf16 v[36:51], v[136:139], v[180:183], v[36:51]\n"
    "v_fma_f32 v3, v3, v255, -v254\n"
    "v_pk_fma_f16 v117, v121, s58, s58\n"
    "v_cvt_pk_bf16_f32 v98, v0, v3\n"
    "v_lshlrev_b32 v0, 16, v99\n"
    "v_pk_fma_f16 v118, v122, s58, s58\n"
    "v_and_b32 v3, s59, v99\n"
    "v_pk_fma_f16 v119, v123, s58, s58\n"
    "v_fma_f32 v0, v0, v255, -v254\n"
    "v_rcp_f16_e32 v120, v116\n"
    "v_fma_f32 v3, v3, v255, -v254\n"
    "v_cvt_pk_bf16_f32 v99, v0, v3\n"
    "v_rcp_f16_e32 v121, v117\n"
    "v_lshlrev_b32 v0, 16, v100\n"
    "v_mfma_f32_32x32x16_bf16 v[52:67], v[152:155], v[96:99], v[52:67]\n"
    "s_waitcnt lgkmcnt(2)\n"
    "v_and_b32 v3, s59, v100\n"
    "v_rcp_f16_e32 v122, v118\n"
    "v_fma_f32 v0, v0, v255, -v254\n"
    "v_fma_f32 v3, v3, v255, -v254\n"
    "v_rcp_f16_e32 v123, v119\n"
    "v_cvt_pk_bf16_f32 v100, v0, v3\n"
    "v_lshlrev_b32 v0, 16, v101\n"
    "v_rcp_f16_sdwa v120, v116 dst_sel:WORD_1 dst_unused:UNUSED_PRESERVE src0_sel:WORD_1\n"
    "v_mfma_f32_32x32x16_bf16 v[4:19], v[156:159], v[168:171], v[4:19]\n"
    "s_waitcnt vmcnt(1)\n"
    "v_and_b32 v3, s59, v101\n"
    "v_fma_f32 v0, v0, v255, -v254\n"
    "v_rcp_f16_sdwa v121, v117 dst_sel:WORD_1 dst_unused:UNUSED_PRESERVE src0_sel:WORD_1\n"
    "v_fma_f32 v3, v3, v255, -v254\n"
    "v_cvt_pk_bf16_f32 v101, v0, v3\n"
    "v_lshlrev_b32 v0, 16, v102\n"
    "v_rcp_f16_sdwa v122, v118 dst_sel:WORD_1 dst_unused:UNUSED_PRESERVE src0_sel:WORD_1\n"
    "v_and_b32 v3, s59, v102\n"
    "v_fma_f32 v0, v0, v255, -v254\n"
    "v_rcp_f16_sdwa v123, v119 dst_sel:WORD_1 dst_unused:UNUSED_PRESERVE src0_sel:WORD_1\n"
    "v_mfma_f32_32x32x16_bf16 v[20:35], v[156:159], v[176:179], v[20:35]\n"
    "v_fma_f32 v3, v3, v255, -v254\n"
    "v_cvt_pk_bf16_f32 v102, v0, v3\n"
    "v_pk_mul_f16 v112, v112, v120\n"
    "v_lshlrev_b32 v0, 16, v103\n"
    "v_pk_mul_f16 v113, v113, v121\n"
    "v_and_b32 v3, s59, v103\n"
    "v_pk_mul_f16 v114, v114, v122\n"
    "v_fma_f32 v0, v0, v255, -v254\n"
    "v_pk_mul_f16 v115, v115, v123\n"
    "v_fma_f32 v3, v3, v255, -v254\n"
    "v_cvt_pk_bf16_f32 v103, v0, v3\n"
    "v_lshlrev_b32 v0, 16, v104\n"
    "s_nop 0\n"
    "v_mfma_f32_32x32x16_bf16 v[52:67], v[140:143], v[100:103], v[52:67]\n"
    "v_and_b32 v3, s59, v104\n"
    "v_fma_f32 v0, v0, v255, -v254\n"
    "v_fma_f32 v3, v3, v255, -v254\n"
    "v_cvt_pk_bf16_f32 v104, v0, v3\n"
    "v_mfma_f32_32x32x16_f16 v[4:19], v[124:127], v[112:115], v[4:19]\n"
    "s_waitcnt vmcnt(0)\n"
    "ds_read_b128 v[124:127], v244\n"
    "v_lshlrev_b32 v0, 16, v105\n"
    "v_and_b32 v3, s59, v105\n"
    "v_fma_f32 v0, v0, v255, -v254\n"
    "v_fma_f32 v3, v3, v255, -v254\n"
    "v_cvt_pk_bf16_f32 v105, v0, v3\n"
    "v_mfma_f32_32x32x16_bf16 v[36:51], v[156:159], v[160:163], v[36:51]\n"
    "s_waitcnt lgkmcnt(2)\n"
    "v_lshlrev_b32 v0, 16, v106\n"
    "v_and_b32 v3, s59, v106\n"
    "v_fma_f32 v0, v0, v255, -v254\n"
    "v_fma_f32 v3, v3, v255, -v254\n"
    "v_cvt_pk_bf16_f32 v106, v0, v3\n"
    "v_mfma_f32_32x32x16_f16 v[20:35], v[128:131], v[112:115], v[20:35]\n"
    "ds_read_b128 v[128:131], v244 offset:1024\n"
    "v_lshlrev_b32 v0, 16, v107\n"
    "v_cvt_pk_bf16_f32 v4, v4, v5\n"
    "v_cvt_pk_bf16_f32 v5, v6, v7\n"
    "v_cvt_pk_bf16_f32 v6, v8, v9\n"
    "v_and_b32 v3, s59, v107\n"
    "v_cvt_pk_bf16_f32 v7, v10, v11\n"
    "v_cvt_pk_bf16_f32 v12, v12, v13\n"
    "v_fma_f32 v0, v0, v255, -v254\n"
    "v_cvt_pk_bf16_f32 v13, v14, v15\n"
    "v_cvt_pk_bf16_f32 v14, v16, v17\n"
    "v_fma_f32 v3, v3, v255, -v254\n"
    "v_cvt_pk_bf16_f32 v15, v18, v19\n"
    "v_permlane16_swap_b32 v4, v12\n"
    "v_cvt_pk_bf16_f32 v107, v0, v3\n"
    "v_permlane16_swap_b32 v5, v13\n"
    "v_permlane16_swap_b32 v6, v14\n"
    "v_mfma_f32_32x32x16_bf16 v[52:67], v[144:147], v[104:107], v[52:67]\n"
    "s_waitcnt lgkmcnt(2)\n"
    "v_permlane16_swap_b32 v7, v15\n"
    "buffer_store_dwordx4 v[4:7], v2, s[40:43], s55 offen nt\n"
    "v_mfma_f32_32x32x16_f16 v[36:51], v[132:135], v[112:115], v[36:51]\n"
    "ds_read_b128 v[132:135], v244 offset:2048\n"
    "buffer_store_dwordx4 v[12:15], v2, s[40:43], s78 offen nt\n"
    "s_sub_u32 s51, s51, 1\n"
    "s_cmp_lg_u32 s51, 0\n"
    "s_cbranch_scc1 L_loop_%=\n"
    "v_cvt_pk_bf16_f32 v20, v20, v21\n"
    "v_cvt_pk_bf16_f32 v21, v22, v23\n"
    "v_cvt_pk_bf16_f32 v22, v24, v25\n"
    "v_cvt_pk_bf16_f32 v23, v26, v27\n"
    "v_cvt_pk_bf16_f32 v28, v28, v29\n"
    "v_cvt_pk_bf16_f32 v29, v30, v31\n"
    "v_cvt_pk_f16_f32 v108, v52, v53\n"
    "v_cvt_pk_bf16_f32 v30, v32, v33\n"
    "v_mfma_f32_32x32x16_bf16 v[68:83], a[0:3], v[84:87], 0\n"
    "v_cvt_pk_f16_f32 v109, v54, v55\n"
    "v_cvt_pk_bf16_f32 v31, v34, v35\n"
    "v_permlane16_swap_b32 v20, v28\n"
    "v_cvt_pk_f16_f32 v110, v56, v57\n"
    "v_cvt_pk_f16_f32 v111, v58, v59\n"
    "v_permlane16_swap_b32 v21, v29\n"
    "v_pk_fma_f16 v116, v108, v108, s57\n"
    "v_pk_fma_f16 v117, v109, v109, s57\n"
    "v_permlane16_swap_b32 v22, v30\n"
    "v_pk_fma_f16 v118, v110, v110, s57\n"
    "v_pk_fma_f16 v119, v111, v111, s57\n"
    "v_permlane16_swap_b32 v23, v31\n"
    "v_pk_mul_f16 v116, v116, v108\n"
    "v_pk_mul_f16 v117, v117, v109\n"
    "v_pk_mul_f16 v118, v118, v110\n"
    "buffer_store_dwordx4 v[20:23], v2, s[40:43], s55 offen offset:64 nt\n"
    "v_pk_mul_f16 v119, v119, v111\n"
    "v_exp_f16_e32 v120, v116\n"
    "v_exp_f16_e32 v121, v117\n"
    "v_mfma_f32_32x32x16_bf16 v[68:83], a[4:7], v[88:91], v[68:83]\n"
    "v_exp_f16_e32 v122, v118\n"
    "v_exp_f16_e32 v123, v119\n"
    "v_exp_f16_sdwa v120, v116 dst_sel:WORD_1 dst_unused:UNUSED_PRESERVE src0_sel:WORD_1\n"
    "v_exp_f16_sdwa v121, v117 dst_sel:WORD_1 dst_unused:UNUSED_PRESERVE src0_sel:WORD_1\n"
    "buffer_store_dwordx4 v[28:31], v2, s[40:43], s78 offen offset:64 nt\n"
    "v_exp_f16_sdwa v122, v118 dst_sel:WORD_1 dst_unused:UNUSED_PRESERVE src0_sel:WORD_1\n"
    "v_exp_f16_sdwa v123, v119 dst_sel:WORD_1 dst_unused:UNUSED_PRESERVE src0_sel:WORD_1\n"
    "v_pk_fma_f16 v116, v120, s58, s58\n"
    "v_pk_fma_f16 v117, v121, s58, s58\n"
    "v_mfma_f32_32x32x16_bf16 v[68:83], a[8:11], v[92:95], v[68:83]\n"
    "v_pk_fma_f16 v118, v122, s58, s58\n"
    "v_pk_fma_f16 v119, v123, s58, s58\n"
    "v_rcp_f16_e32 v120, v116\n"
    "v_rcp_f16_e32 v121, v117\n"
    "v_rcp_f16_e32 v122, v118\n"
    "v_rcp_f16_e32 v123, v119\n"
    "v_rcp_f16_sdwa v120, v116 dst_sel:WORD_1 dst_unused:UNUSED_PRESERVE src0_sel:WORD_1\n"
    "v_cvt_pk_bf16_f32 v36, v36, v37\n"
    "v_cvt_pk_bf16_f32 v37, v38, v39\n"
    "v_rcp_f16_sdwa v121, v117 dst_sel:WORD_1 dst_unused:UNUSED_PRESERVE src0_sel:WORD_1\n"
    "v_cvt_pk_bf16_f32 v38, v40, v41\n"
    "v_mfma_f32_32x32x16_bf16 v[68:83], a[12:15], v[96:99], v[68:83]\n"
    "v_rcp_f16_sdwa v122, v118 dst_sel:WORD_1 dst_unused:UNUSED_PRESERVE src0_sel:WORD_1\n"
    "v_cvt_pk_bf16_f32 v39, v42, v43\n"
    "v_cvt_pk_bf16_f32 v44, v44, v45\n"
    "v_rcp_f16_sdwa v123, v119 dst_sel:WORD_1 dst_unused:UNUSED_PRESERVE src0_sel:WORD_1\n"
    "v_cvt_pk_bf16_f32 v45, v46, v47\n"
    "v_pk_mul_f16 v108, v108, v120\n"
    "v_cvt_pk_bf16_f32 v46, v48, v49\n"
    "v_pk_mul_f16 v109, v109, v121\n"
    "v_cvt_pk_bf16_f32 v47, v50, v51\n"
    "v_pk_mul_f16 v110, v110, v122\n"
    "v_permlane16_swap_b32 v36, v44\n"
    "v_pk_mul_f16 v111, v111, v123\n"
    "v_cvt_pk_f16_f32 v112, v60, v61\n"
    "v_cvt_pk_f16_f32 v113, v62, v63\n"
    "v_permlane16_swap_b32 v37, v45\n"
    "v_cvt_pk_f16_f32 v114, v64, v65\n"
    "v_cvt_pk_f16_f32 v115, v66, v67\n"
    "v_permlane16_swap_b32 v38, v46\n"
    "v_pk_fma_f16 v116, v112, v112, s57\n"
    "v_pk_fma_f16 v117, v113, v113, s57\n"
    "v_pk_fma_f16 v118, v114, v114, s57\n"
    "v_mfma_f32_32x32x16_bf16 v[68:83], a[16:19], v[100:103], v[68:83]\n"
    "s_waitcnt lgkmcnt(2)\n"
    "v_permlane16_swap_b32 v39, v47\n"
    "v_pk_fma_f16 v119, v115, v115, s57\n"
    "v_pk_mul_f16 v116, v116, v112\n"
    "v_pk_mul_f16 v117, v117, v113\n"
    "buffer_store_dwordx4 v[36:39], v2, s[40:43], s55 offen offset:128 nt\n"
    "v_pk_mul_f16 v118, v118, v114\n"
    "v_pk_mul_f16 v119, v119, v115\n"
    "v_exp_f16_e32 v120, v116\n"
    "v_mfma_f32_32x32x16_f16 v[4:19], v[124:127], v[108:111], 0\n"
    "ds_read_b128 v[124:127], v244 offset:3072\n"
    "v_exp_f16_e32 v121, v117\n"
    "v_exp_f16_e32 v122, v118\n"
    "v_exp_f16_e32 v123, v119\n"
    "v_exp_f16_sdwa v120, v116 dst_sel:WORD_1 dst_unused:UNUSED_PRESERVE src0_sel:WORD_1\n"
    "v_mfma_f32_32x32x16_bf16 v[68:83], a[20:23], v[104:107], v[68:83]\n"
    "s_waitcnt lgkmcnt(2)\n"
    "v_exp_f16_sdwa v121, v117 dst_sel:WORD_1 dst_unused:UNUSED_PRESERVE src0_sel:WORD_1\n"
    "v_exp_f16_sdwa v122, v118 dst_sel:WORD_1 dst_unused:UNUSED_PRESERVE src0_sel:WORD_1\n"
    "buffer_store_dwordx4 v[44:47], v2, s[40:43], s78 offen offset:128 nt\n"
    "v_exp_f16_sdwa v123, v119 dst_sel:WORD_1 dst_unused:UNUSED_PRESERVE src0_sel:WORD_1\n"
    "v_pk_fma_f16 v116, v120, s58, s58\n"
    "v_pk_fma_f16 v117, v121, s58, s58\n"
    "v_pk_fma_f16 v118, v122, s58, s58\n"
    "v_pk_fma_f16 v119, v123, s58, s58\n"
    "v_rcp_f16_e32 v120, v116\n"
    "v_rcp_f16_e32 v121, v117\n"
    "v_mfma_f32_32x32x16_f16 v[20:35], v[128:131], v[108:111], 0\n"
    "s_waitcnt lgkmcnt(1)\n"
    "ds_read_b128 v[128:131], v244 offset:4096\n"
    "v_rcp_f16_e32 v122, v118\n"
    "v_rcp_f16_e32 v123, v119\n"
    "v_rcp_f16_sdwa v120, v116 dst_sel:WORD_1 dst_unused:UNUSED_PRESERVE src0_sel:WORD_1\n"
    "v_rcp_f16_sdwa v121, v117 dst_sel:WORD_1 dst_unused:UNUSED_PRESERVE src0_sel:WORD_1\n"
    "v_rcp_f16_sdwa v122, v118 dst_sel:WORD_1 dst_unused:UNUSED_PRESERVE src0_sel:WORD_1\n"
    "v_rcp_f16_sdwa v123, v119 dst_sel:WORD_1 dst_unused:UNUSED_PRESERVE src0_sel:WORD_1\n"
    "v_pk_mul_f16 v112, v112, v120\n"
    "v_mfma_f32_32x32x16_f16 v[36:51], v[132:135], v[108:111], 0\n"
    "s_waitcnt lgkmcnt(1)\n"
    "ds_read_b128 v[132:135], v244 offset:5120\n"
    "v_pk_mul_f16 v113, v113, v121\n"
    "v_pk_mul_f16 v114, v114, v122\n"
    "v_pk_mul_f16 v115, v115, v123\n"
    "v_cvt_pk_f16_f32 v108, v68, v69\n"
    "v_cvt_pk_f16_f32 v109, v70, v71\n"
    "v_mfma_f32_32x32x16_f16 v[4:19], v[124:127], v[112:115], v[4:19]\n"
    "ds_read_b128 v[124:127], v244 offset:6144\n"
    "v_cvt_pk_f16_f32 v110, v72, v73\n"
    "v_cvt_pk_f16_f32 v111, v74, v75\n"
    "v_pk_fma_f16 v116, v108, v108, s57\n"
    "v_pk_fma_f16 v117, v109, v109, s57\n"
    "v_pk_fma_f16 v118, v110, v110, s57\n"
    "v_pk_fma_f16 v119, v111, v111, s57\n"
    "v_pk_mul_f16 v116, v116, v108\n"
    "v_mfma_f32_32x32x16_bf16 v[52:67], a[24:27], v[84:87], 0\n"
    "s_waitcnt lgkmcnt(2)\n"
    "v_pk_mul_f16 v117, v117, v109\n"
    "v_pk_mul_f16 v118, v118, v110\n"
    "v_pk_mul_f16 v119, v119, v111\n"
    "v_exp_f16_e32 v120, v116\n"
    "v_exp_f16_e32 v121, v117\n"
    "v_exp_f16_e32 v122, v118\n"
    "v_mfma_f32_32x32x16_f16 v[20:35], v[128:131], v[112:115], v[20:35]\n"
    "ds_read_b128 v[128:131], v244 offset:7168\n"
    "v_exp_f16_e32 v123, v119\n"
    "v_exp_f16_sdwa v120, v116 dst_sel:WORD_1 dst_unused:UNUSED_PRESERVE src0_sel:WORD_1\n"
    "v_exp_f16_sdwa v121, v117 dst_sel:WORD_1 dst_unused:UNUSED_PRESERVE src0_sel:WORD_1\n"
    "v_exp_f16_sdwa v122, v118 dst_sel:WORD_1 dst_unused:UNUSED_PRESERVE src0_sel:WORD_1\n"
    "v_mfma_f32_32x32x16_bf16 v[52:67], a[28:31], v[88:91], v[52:67]\n"
    "s_waitcnt lgkmcnt(2)\n"
    "v_exp_f16_sdwa v123, v119 dst_sel:WORD_1 dst_unused:UNUSED_PRESERVE src0_sel:WORD_1\n"
    "v_pk_fma_f16 v116, v120, s58, s58\n"
    "v_pk_fma_f16 v117, v121, s58, s58\n"
    "v_pk_fma_f16 v118, v122, s58, s58\n"
    "v_pk_fma_f16 v119, v123, s58, s58\n"
    "v_rcp_f16_e32 v120, v116\n"
    "v_mfma_f32_32x32x16_f16 v[36:51], v[132:135], v[112:115], v[36:51]\n"
    "ds_read_b128 v[132:135], v244 offset:8192\n"
    "v_rcp_f16_e32 v121, v117\n"
    "v_rcp_f16_e32 v122, v118\n"
    "v_rcp_f16_e32 v123, v119\n"
    "v_rcp_f16_sdwa v120, v116 dst_sel:WORD_1 dst_unused:UNUSED_PRESERVE src0_sel:WORD_1\n"
    "v_mfma_f32_32x32x16_bf16 v[52:67], a[32:35], v[92:95], v[52:67]\n"
    "v_rcp_f16_sdwa v121, v117 dst_sel:WORD_1 dst_unused:UNUSED_PRESERVE src0_sel:WORD_1\n"
    "v_rcp_f16_sdwa v122, v118 dst_sel:WORD_1 dst_unused:UNUSED_PRESERVE src0_sel:WORD_1\n"
    "v_rcp_f16_sdwa v123, v119 dst_sel:WORD_1 dst_unused:UNUSED_PRESERVE src0_sel:WORD_1\n"
    "v_pk_mul_f16 v108, v108, v120\n"
    "v_pk_mul_f16 v109, v109, v121\n"
    "v_pk_mul_f16 v110, v110, v122\n"
    "v_mfma_f32_32x32x16_bf16 v[52:67], a[36:39], v[96:99], v[52:67]\n"
    "v_pk_mul_f16 v111, v111, v123\n"
    "v_cvt_pk_f16_f32 v112, v76, v77\n"
    "v_cvt_pk_f16_f32 v113, v78, v79\n"
    "v_cvt_pk_f16_f32 v114, v80, v81\n"
    "v_cvt_pk_f16_f32 v115, v82, v83\n"
    "v_pk_fma_f16 v116, v112, v112, s57\n"
    "v_pk_fma_f16 v117, v113, v113, s57\n"
    "v_pk_fma_f16 v118, v114, v114, s57\n"
    "v_mfma_f32_32x32x16_bf16 v[52:67], a[40:43], v[100:103], v[52:67]\n"
    "v_pk_fma_f16 v119, v115, v115, s57\n"
    "v_pk_mul_f16 v116, v116, v112\n"
    "v_pk_mul_f16 v117, v117, v113\n"
    "v_pk_mul_f16 v118, v118, v114\n"
    "v_pk_mul_f16 v119, v119, v115\n"
    "v_exp_f16_e32 v120, v116\n"
    "v_exp_f16_e32 v121, v117\n"
    "v_mfma_f32_32x32x16_bf16 v[52:67], a[44:47], v[104:107], v[52:67]\n"
    "s_waitcnt lgkmcnt(2)\n"
    "v_exp_f16_e32 v122, v118\n"
    "v_exp_f16_e32 v123, v119\n"
    "v_exp_f16_sdwa v120, v116 dst_sel:WORD_1 dst_unused:UNUSED_PRESERVE src0_sel:WORD_1\n"
    "v_exp_f16_sdwa v121, v117 dst_sel:WORD_1 dst_unused:UNUSED_PRESERVE src0_sel:WORD_1\n"
    "v_exp_f16_sdwa v122, v118 dst_sel:WORD_1 dst_unused:UNUSED_PRESERVE src0_sel:WORD_1\n"
    "v_exp_f16_sdwa v123, v119 dst_sel:WORD_1 dst_unused:UNUSED_PRESERVE src0_sel:WORD_1\n"
    "v_mfma_f32_32x32x16_f16 v[4:19], v[124:127], v[108:111], v[4:19]\n"
    "s_waitcnt lgkmcnt(1)\n"
    "ds_read_b128 v[124:127], v244 offset:9216\n"
    "v_pk_fma_f16 v116, v120, s58, s58\n"
    "v_pk_fma_f16 v117, v121, s58, s58\n"
    "v_pk_fma_f16 v118, v122, s58, s58\n"
    "v_pk_fma_f16 v119, v123, s58, s58\n"
    "v_rcp_f16_e32 v120, v116\n"
    "v_rcp_f16_e32 v121, v117\n"
    "v_rcp_f16_e32 v122, v118\n"
    "v_mfma_f32_32x32x16_f16 v[20:35], v[128:131], v[108:111], v[20:35]\n"
    "s_waitcnt lgkmcnt(1)\n"
    "ds_read_b128 v[128:131], v244 offset:10240\n"
    "v_rcp_f16_e32 v123, v119\n"
    "v_rcp_f16_sdwa v120, v116 dst_sel:WORD_1 dst_unused:UNUSED_PRESERVE src0_sel:WORD_1\n"
    "v_rcp_f16_sdwa v121, v117 dst_sel:WORD_1 dst_unused:UNUSED_PRESERVE src0_sel:WORD_1\n"
    "v_rcp_f16_sdwa v122, v118 dst_sel:WORD_1 dst_unused:UNUSED_PRESERVE src0_sel:WORD_1\n"
    "v_rcp_f16_sdwa v123, v119 dst_sel:WORD_1 dst_unused:UNUSED_PRESERVE src0_sel:WORD_1\n"
    "v_mfma_f32_32x32x16_f16 v[36:51], v[132:135], v[108:111], v[36:51]\n"
    "ds_read_b128 v[132:135], v244 offset:11264\n"
    "v_pk_mul_f16 v112, v112, v120\n"
    "v_pk_mul_f16 v113, v113, v121\n"
    "v_pk_mul_f16 v114, v114, v122\n"
    "v_pk_mul_f16 v115, v115, v123\n"
    "v_cvt_pk_f16_f32 v108, v52, v53\n"
    "v_cvt_pk_f16_f32 v109, v54, v55\n"
    "s_waitcnt lgkmcnt(2)\n"
    "v_mfma_f32_32x32x16_f16 v[4:19], v[124:127], v[112:115], v[4:19]\n"
    "ds_read_b128 v[124:127], v244 offset:12288\n"
    "v_cvt_pk_f16_f32 v110, v56, v57\n"
    "v_cvt_pk_f16_f32 v111, v58, v59\n"
    "v_pk_fma_f16 v116, v108, v108, s57\n"
    "v_pk_fma_f16 v117, v109, v109, s57\n"
    "v_pk_fma_f16 v118, v110, v110, s57\n"
    "v_pk_fma_f16 v119, v111, v111, s57\n"
    "v_pk_mul_f16 v116, v116, v108\n"
    "v_mfma_f32_32x32x16_bf16 v[68:83], a[48:51], v[84:87], 0\n"
    "s_waitcnt lgkmcnt(2)\n"
    "v_pk_mul_f16 v117, v117, v109\n"
    "v_pk_mul_f16 v118, v118, v110\n"
    "v_pk_mul_f16 v119, v119, v111\n"
    "v_exp_f16_e32 v120, v116\n"
    "v_exp_f16_e32 v121, v117\n"
    "v_exp_f16_e32 v122, v118\n"
    "v_mfma_f32_32x32x16_f16 v[20:35], v[128:131], v[112:115], v[20:35]\n"
    "ds_read_b128 v[128:131], v244 offset:13312\n"
    "v_exp_f16_e32 v123, v119\n"
    "v_exp_f16_sdwa v120, v116 dst_sel:WORD_1 dst_unused:UNUSED_PRESERVE src0_sel:WORD_1\n"
    "v_exp_f16_sdwa v121, v117 dst_sel:WORD_1 dst_unused:UNUSED_PRESERVE src0_sel:WORD_1\n"
    "v_exp_f16_sdwa v122, v118 dst_sel:WORD_1 dst_unused:UNUSED_PRESERVE src0_sel:WORD_1\n"
    "v_mfma_f32_32x32x16_bf16 v[68:83], a[52:55], v[88:91], v[68:83]\n"
    "s_waitcnt lgkmcnt(2)\n"
    "v_exp_f16_sdwa v123, v119 dst_sel:WORD_1 dst_unused:UNUSED_PRESERVE src0_sel:WORD_1\n"
    "v_pk_fma_f16 v116, v120, s58, s58\n"
    "v_pk_fma_f16 v117, v121, s58, s58\n"
    "v_pk_fma_f16 v118, v122, s58, s58\n"
    "v_pk_fma_f16 v119, v123, s58, s58\n"
    "v_rcp_f16_e32 v120, v116\n"
    "v_mfma_f32_32x32x16_f16 v[36:51], v[132:135], v[112:115], v[36:51]\n"
    "ds_read_b128 v[132:135], v244 offset:14336\n"
    "v_rcp_f16_e32 v121, v117\n"
    "v_rcp_f16_e32 v122, v118\n"
    "v_rcp_f16_e32 v123, v119\n"
    "v_rcp_f16_sdwa v120, v116 dst_sel:WORD_1 dst_unused:UNUSED_PRESERVE src0_sel:WORD_1\n"
    "v_mfma_f32_32x32x16_bf16 v[68:83], a[56:59], v[92:95], v[68:83]\n"
    "v_rcp_f16_sdwa v121, v117 dst_sel:WORD_1 dst_unused:UNUSED_PRESERVE src0_sel:WORD_1\n"
    "v_rcp_f16_sdwa v122, v118 dst_sel:WORD_1 dst_unused:UNUSED_PRESERVE src0_sel:WORD_1\n"
    "v_rcp_f16_sdwa v123, v119 dst_sel:WORD_1 dst_unused:UNUSED_PRESERVE src0_sel:WORD_1\n"
    "v_pk_mul_f16 v108, v108, v120\n"
    "v_pk_mul_f16 v109, v109, v121\n"
    "v_pk_mul_f16 v110, v110, v122\n"
    "v_mfma_f32_32x32x16_bf16 v[68:83], a[60:63], v[96:99], v[68:83]\n"
    "v_pk_mul_f16 v111, v111, v123\n"
    "v_cvt_pk_f16_f32 v112, v60, v61\n"
    "v_cvt_pk_f16_f32 v113, v62, v63\n"
    "v_cvt_pk_f16_f32 v114, v64, v65\n"
    "v_cvt_pk_f16_f32 v115, v66, v67\n"
    "v_pk_fma_f16 v116, v112, v112, s57\n"
    "v_pk_fma_f16 v117, v113, v113, s57\n"
    "v_pk_fma_f16 v118, v114, v114, s57\n"
    "v_mfma_f32_32x32x16_bf16 v[68:83], a[64:67], v[100:103], v[68:83]\n"
    "v_pk_fma_f16 v119, v115, v115, s57\n"
    "v_pk_mul_f16 v116, v116, v112\n"
    "v_pk_mul_f16 v117, v117, v113\n"
    "v_pk_mul_f16 v118, v118, v114\n"
    "v_pk_mul_f16 v119, v119, v115\n"
    "v_exp_f16_e32 v120, v116\n"
    "v_exp_f16_e32 v121, v117\n"
    "v_mfma_f32_32x32x16_bf16 v[68:83], a[68:71], v[104:107], v[68:83]\n"
    "s_waitcnt lgkmcnt(2)\n"
    "v_exp_f16_e32 v122, v118\n"
    "v_exp_f16_e32 v123, v119\n"
    "v_exp_f16_sdwa v120, v116 dst_sel:WORD_1 dst_unused:UNUSED_PRESERVE src0_sel:WORD_1\n"
    "v_exp_f16_sdwa v121, v117 dst_sel:WORD_1 dst_unused:UNUSED_PRESERVE src0_sel:WORD_1\n"
    "v_exp_f16_sdwa v122, v118 dst_sel:WORD_1 dst_unused:UNUSED_PRESERVE src0_sel:WORD_1\n"
    "v_exp_f16_sdwa v123, v119 dst_sel:WORD_1 dst_unused:UNUSED_PRESERVE src0_sel:WORD_1\n"
    "v_mfma_f32_32x32x16_f16 v[4:19], v[124:127], v[108:111], v[4:19]\n"
    "s_waitcnt lgkmcnt(1)\n"
    "ds_read_b128 v[124:127], v244 offset:15360\n"
    "v_pk_fma_f16 v116, v120, s58, s58\n"
    "v_pk_fma_f16 v117, v121, s58, s58\n"
    "v_pk_fma_f16 v118, v122, s58, s58\n"
    "v_pk_fma_f16 v119, v123, s58, s58\n"
    "v_rcp_f16_e32 v120, v116\n"
    "v_rcp_f16_e32 v121, v117\n"
    "v_rcp_f16_e32 v122, v118\n"
    "v_mfma_f32_32x32x16_f16 v[20:35], v[128:131], v[108:111], v[20:35]\n"
    "s_waitcnt lgkmcnt(1)\n"
    "ds_read_b128 v[128:131], v244 offset:16384\n"
    "v_rcp_f16_e32 v123, v119\n"
    "v_rcp_f16_sdwa v120, v116 dst_sel:WORD_1 dst_unused:UNUSED_PRESERVE src0_sel:WORD_1\n"
    "v_rcp_f16_sdwa v121, v117 dst_sel:WORD_1 dst_unused:UNUSED_PRESERVE src0_sel:WORD_1\n"
    "v_rcp_f16_sdwa v122, v118 dst_sel:WORD_1 dst_unused:UNUSED_PRESERVE src0_sel:WORD_1\n"
    "v_rcp_f16_sdwa v123, v119 dst_sel:WORD_1 dst_unused:UNUSED_PRESERVE src0_sel:WORD_1\n"
    "v_mfma_f32_32x32x16_f16 v[36:51], v[132:135], v[108:111], v[36:51]\n"
    "ds_read_b128 v[132:135], v244 offset:17408\n"
    "v_pk_mul_f16 v112, v112, v120\n"
    "v_pk_mul_f16 v113, v113, v121\n"
    "v_pk_mul_f16 v114, v114, v122\n"
    "v_pk_mul_f16 v115, v115, v123\n"
    "v_cvt_pk_f16_f32 v108, v68, v69\n"
    "v_cvt_pk_f16_f32 v109, v70, v71\n"
    "s_waitcnt lgkmcnt(2)\n"
    "v_mfma_f32_32x32x16_f16 v[4:19], v[124:127], v[112:115], v[4:19]\n"
    "ds_read_b128 v[124:127], v244 offset:18432\n"
    "v_cvt_pk_f16_f32 v110, v72, v73\n"
    "v_cvt_pk_f16_f32 v111, v74, v75\n"
    "v_pk_fma_f16 v116, v108, v108, s57\n"
    "v_pk_fma_f16 v117, v109, v109, s57\n"
    "v_pk_fma_f16 v118, v110, v110, s57\n"
    "v_pk_fma_f16 v119, v111, v111, s57\n"
    "v_pk_mul_f16 v116, v116, v108\n"
    "v_mfma_f32_32x32x16_bf16 v[52:67], a[72:75], v[84:87], 0\n"
    "s_waitcnt lgkmcnt(2)\n"
    "v_pk_mul_f16 v117, v117, v109\n"
    "v_pk_mul_f16 v118, v118, v110\n"
    "v_pk_mul_f16 v119, v119, v111\n"
    "v_exp_f16_e32 v120, v116\n"
    "v_exp_f16_e32 v121, v117\n"
    "v_exp_f16_e32 v122, v118\n"
    "v_mfma_f32_32x32x16_f16 v[20:35], v[128:131], v[112:115], v[20:35]\n"
    "ds_read_b128 v[128:131], v244 offset:19456\n"
    "v_exp_f16_e32 v123, v119\n"
    "v_exp_f16_sdwa v120, v116 dst_sel:WORD_1 dst_unused:UNUSED_PRESERVE src0_sel:WORD_1\n"
    "v_exp_f16_sdwa v121, v117 dst_sel:WORD_1 dst_unused:UNUSED_PRESERVE src0_sel:WORD_1\n"
    "v_exp_f16_sdwa v122, v118 dst_sel:WORD_1 dst_unused:UNUSED_PRESERVE src0_sel:WORD_1\n"
    "v_mfma_f32_32x32x16_bf16 v[52:67], a[76:79], v[88:91], v[52:67]\n"
    "s_waitcnt lgkmcnt(2)\n"
    "v_exp_f16_sdwa v123, v119 dst_sel:WORD_1 dst_unused:UNUSED_PRESERVE src0_sel:WORD_1\n"
    "v_pk_fma_f16 v116, v120, s58, s58\n"
    "v_pk_fma_f16 v117, v121, s58, s58\n"
    "v_pk_fma_f16 v118, v122, s58, s58\n"
    "v_pk_fma_f16 v119, v123, s58, s58\n"
    "v_rcp_f16_e32 v120, v116\n"
    "v_mfma_f32_32x32x16_f16 v[36:51], v[132:135], v[112:115], v[36:51]\n"
    "ds_read_b128 v[132:135], v244 offset:20480\n"
    "v_rcp_f16_e32 v121, v117\n"
    "v_rcp_f16_e32 v122, v118\n"
    "v_rcp_f16_e32 v123, v119\n"
    "v_rcp_f16_sdwa v120, v116 dst_sel:WORD_1 dst_unused:UNUSED_PRESERVE src0_sel:WORD_1\n"
    "v_mfma_f32_32x32x16_bf16 v[52:67], a[80:83], v[92:95], v[52:67]\n"
    "v_rcp_f16_sdwa v121, v117 dst_sel:WORD_1 dst_unused:UNUSED_PRESERVE src0_sel:WORD_1\n"
    "v_rcp_f16_sdwa v122, v118 dst_sel:WORD_1 dst_unused:UNUSED_PRESERVE src0_sel:WORD_1\n"
    "v_rcp_f16_sdwa v123, v119 dst_sel:WORD_1 dst_unused:UNUSED_PRESERVE src0_sel:WORD_1\n"
    "v_pk_mul_f16 v108, v108, v120\n"
    "v_pk_mul_f16 v109, v109, v121\n"
    "v_pk_mul_f16 v110, v110, v122\n"
    "v_mfma_f32_32x32x16_bf16 v[52:67], a[84:87], v[96:99], v[52:67]\n"
    "v_pk_mul_f16 v111, v111, v123\n"
    "v_cvt_pk_f16_f32 v112, v76, v77\n"
    "v_cvt_pk_f16_f32 v113, v78, v79\n"
    "v_cvt_pk_f16_f32 v114, v80, v81\n"
    "v_cvt_pk_f16_f32 v115, v82, v83\n"
    "v_pk_fma_f16 v116, v112, v112, s57\n"
    "v_pk_fma_f16 v117, v113, v113, s57\n"
    "v_pk_fma_f16 v118, v114, v114, s57\n"
    "v_mfma_f32_32x32x16_bf16 v[52:67], a[88:91], v[100:103], v[52:67]\n"
    "v_pk_fma_f16 v119, v115, v115, s57\n"
    "v_pk_mul_f16 v116, v116, v112\n"
    "v_pk_mul_f16 v117, v117, v113\n"
    "v_pk_mul_f16 v118, v118, v114\n"
    "v_pk_mul_f16 v119, v119, v115\n"
    "v_exp_f16_e32 v120, v116\n"
    "v_exp_f16_e32 v121, v117\n"
    "v_mfma_f32_32x32x16_bf16 v[52:67], a[92:95], v[104:107], v[52:67]\n"
    "s_waitcnt lgkmcnt(2)\n"
    "v_exp_f16_e32 v122, v118\n"
    "v_exp_f16_e32 v123, v119\n"
    "v_exp_f16_sdwa v120, v116 dst_sel:WORD_1 dst_unused:UNUSED_PRESERVE src0_sel:WORD_1\n"
    "v_exp_f16_sdwa v121, v117 dst_sel:WORD_1 dst_unused:UNUSED_PRESERVE src0_sel:WORD_1\n"
    "v_exp_f16_sdwa v122, v118 dst_sel:WORD_1 dst_unused:UNUSED_PRESERVE src0_sel:WORD_1\n"
    "v_exp_f16_sdwa v123, v119 dst_sel:WORD_1 dst_unused:UNUSED_PRESERVE src0_sel:WORD_1\n"
    "v_mfma_f32_32x32x16_f16 v[4:19], v[124:127], v[108:111], v[4:19]\n"
    "s_waitcnt lgkmcnt(1)\n"
    "ds_read_b128 v[124:127], v244 offset:21504\n"
    "v_pk_fma_f16 v116, v120, s58, s58\n"
    "v_pk_fma_f16 v117, v121, s58, s58\n"
    "v_pk_fma_f16 v118, v122, s58, s58\n"
    "v_pk_fma_f16 v119, v123, s58, s58\n"
    "v_rcp_f16_e32 v120, v116\n"
    "v_rcp_f16_e32 v121, v117\n"
    "v_rcp_f16_e32 v122, v118\n"
    "v_mfma_f32_32x32x16_f16 v[20:35], v[128:131], v[108:111], v[20:35]\n"
    "s_waitcnt lgkmcnt(1)\n"
    "ds_read_b128 v[128:131], v244 offset:22528\n"
    "v_rcp_f16_e32 v123, v119\n"
    "v_rcp_f16_sdwa v120, v116 dst_sel:WORD_1 dst_unused:UNUSED_PRESERVE src0_sel:WORD_1\n"
    "v_rcp_f16_sdwa v121, v117 dst_sel:WORD_1 dst_unused:UNUSED_PRESERVE src0_sel:WORD_1\n"
    "v_rcp_f16_sdwa v122, v118 dst_sel:WORD_1 dst_unused:UNUSED_PRESERVE src0_sel:WORD_1\n"
    "v_rcp_f16_sdwa v123, v119 dst_sel:WORD_1 dst_unused:UNUSED_PRESERVE src0_sel:WORD_1\n"
    "v_mfma_f32_32x32x16_f16 v[36:51], v[132:135], v[108:111], v[36:51]\n"
    "ds_read_b128 v[132:135], v244 offset:23552\n"
    "v_pk_mul_f16 v112, v112, v120\n"
    "v_pk_mul_f16 v113, v113, v121\n"
    "v_pk_mul_f16 v114, v114, v122\n"
    "v_pk_mul_f16 v115, v115, v123\n"
    "v_cvt_pk_f16_f32 v108, v52, v53\n"
    "v_cvt_pk_f16_f32 v109, v54, v55\n"
    "s_waitcnt lgkmcnt(2)\n"
    "v_mfma_f32_32x32x16_f16 v[4:19], v[124:127], v[112:115], v[4:19]\n"
    "ds_read_b128 v[124:127], v244 offset:24576\n"
    "v_cvt_pk_f16_f32 v110, v56, v57\n"
    "v_cvt_pk_f16_f32 v111, v58, v59\n"
    "v_pk_fma_f16 v116, v108, v108, s57\n"
    "v_pk_fma_f16 v117, v109, v109, s57\n"
    "v_pk_fma_f16 v118, v110, v110, s57\n"
    "v_pk_fma_f16 v119, v111, v111, s57\n"
    "v_pk_mul_f16 v116, v116, v108\n"
    "v_mfma_f32_32x32x16_bf16 v[68:83], a[96:99], v[84:87], 0\n"
    "s_waitcnt lgkmcnt(2)\n"
    "v_pk_mul_f16 v117, v117, v109\n"
    "v_pk_mul_f16 v118, v118, v110\n"
    "v_pk_mul_f16 v119, v119, v111\n"
    "v_exp_f16_e32 v120, v116\n"
    "v_exp_f16_e32 v121, v117\n"
    "v_exp_f16_e32 v122, v118\n"
    "v_mfma_f32_32x32x16_f16 v[20:35], v[128:131], v[112:115], v[20:35]\n"
    "ds_read_b128 v[128:131], v244 offset:25600\n"
    "v_exp_f16_e32 v123, v119\n"
    "v_exp_f16_sdwa v120, v116 dst_sel:WORD_1 dst_unused:UNUSED_PRESERVE src0_sel:WORD_1\n"
    "v_exp_f16_sdwa v121, v117 dst_sel:WORD_1 dst_unused:UNUSED_PRESERVE src0_sel:WORD_1\n"
    "v_exp_f16_sdwa v122, v118 dst_sel:WORD_1 dst_unused:UNUSED_PRESERVE src0_sel:WORD_1\n"
    "v_mfma_f32_32x32x16_bf16 v[68:83], a[100:103], v[88:91], v[68:83]\n"
    "s_waitcnt lgkmcnt(2)\n"
    "v_exp_f16_sdwa v123, v119 dst_sel:WORD_1 dst_unused:UNUSED_PRESERVE src0_sel:WORD_1\n"
    "v_pk_fma_f16 v116, v120, s58, s58\n"
    "v_pk_fma_f16 v117, v121, s58, s58\n"
    "v_pk_fma_f16 v118, v122, s58, s58\n"
    "v_pk_fma_f16 v119, v123, s58, s58\n"
    "v_rcp_f16_e32 v120, v116\n"
    "v_mfma_f32_32x32x16_f16 v[36:51], v[132:135], v[112:115], v[36:51]\n"
    "ds_read_b128 v[132:135], v244 offset:26624\n"
    "v_rcp_f16_e32 v121, v117\n"
    "v_rcp_f16_e32 v122, v118\n"
    "v_rcp_f16_e32 v123, v119\n"
    "v_rcp_f16_sdwa v120, v116 dst_sel:WORD_1 dst_unused:UNUSED_PRESERVE src0_sel:WORD_1\n"
    "v_mfma_f32_32x32x16_bf16 v[68:83], a[104:107], v[92:95], v[68:83]\n"
    "v_rcp_f16_sdwa v121, v117 dst_sel:WORD_1 dst_unused:UNUSED_PRESERVE src0_sel:WORD_1\n"
    "v_rcp_f16_sdwa v122, v118 dst_sel:WORD_1 dst_unused:UNUSED_PRESERVE src0_sel:WORD_1\n"
    "v_rcp_f16_sdwa v123, v119 dst_sel:WORD_1 dst_unused:UNUSED_PRESERVE src0_sel:WORD_1\n"
    "v_pk_mul_f16 v108, v108, v120\n"
    "v_pk_mul_f16 v109, v109, v121\n"
    "v_pk_mul_f16 v110, v110, v122\n"
    "v_mfma_f32_32x32x16_bf16 v[68:83], a[108:111], v[96:99], v[68:83]\n"
    "v_pk_mul_f16 v111, v111, v123\n"
    "v_cvt_pk_f16_f32 v112, v60, v61\n"
    "v_cvt_pk_f16_f32 v113, v62, v63\n"
    "v_cvt_pk_f16_f32 v114, v64, v65\n"
    "v_cvt_pk_f16_f32 v115, v66, v67\n"
    "v_pk_fma_f16 v116, v112, v112, s57\n"
    "v_pk_fma_f16 v117, v113, v113, s57\n"
    "v_pk_fma_f16 v118, v114, v114, s57\n"
    "v_mfma_f32_32x32x16_bf16 v[68:83], a[112:115], v[100:103], v[68:83]\n"
    "v_pk_fma_f16 v119, v115, v115, s57\n"
    "v_pk_mul_f16 v116, v116, v112\n"
    "v_pk_mul_f16 v117, v117, v113\n"
    "v_pk_mul_f16 v118, v118, v114\n"
    "v_pk_mul_f16 v119, v119, v115\n"
    "v_exp_f16_e32 v120, v116\n"
    "v_exp_f16_e32 v121, v117\n"
    "v_mfma_f32_32x32x16_bf16 v[68:83], a[116:119], v[104:107], v[68:83]\n"
    "s_waitcnt lgkmcnt(2)\n"
    "v_exp_f16_e32 v122, v118\n"
    "v_exp_f16_e32 v123, v119\n"
    "v_exp_f16_sdwa v120, v116 dst_sel:WORD_1 dst_unused:UNUSED_PRESERVE src0_sel:WORD_1\n"
    "v_exp_f16_sdwa v121, v117 dst_sel:WORD_1 dst_unused:UNUSED_PRESERVE src0_sel:WORD_1\n"
    "v_exp_f16_sdwa v122, v118 dst_sel:WORD_1 dst_unused:UNUSED_PRESERVE src0_sel:WORD_1\n"
    "v_exp_f16_sdwa v123, v119 dst_sel:WORD_1 dst_unused:UNUSED_PRESERVE src0_sel:WORD_1\n"
    "v_mfma_f32_32x32x16_f16 v[4:19], v[124:127], v[108:111], v[4:19]\n"
    "s_waitcnt lgkmcnt(1)\n"
    "ds_read_b128 v[124:127], v244 offset:27648\n"
    "v_pk_fma_f16 v116, v120, s58, s58\n"
    "v_pk_fma_f16 v117, v121, s58, s58\n"
    "v_pk_fma_f16 v118, v122, s58, s58\n"
    "v_pk_fma_f16 v119, v123, s58, s58\n"
    "v_rcp_f16_e32 v120, v116\n"
    "v_rcp_f16_e32 v121, v117\n"
    "v_rcp_f16_e32 v122, v118\n"
    "v_mfma_f32_32x32x16_f16 v[20:35], v[128:131], v[108:111], v[20:35]\n"
    "s_waitcnt lgkmcnt(1)\n"
    "ds_read_b128 v[128:131], v244 offset:28672\n"
    "v_rcp_f16_e32 v123, v119\n"
    "v_rcp_f16_sdwa v120, v116 dst_sel:WORD_1 dst_unused:UNUSED_PRESERVE src0_sel:WORD_1\n"
    "v_rcp_f16_sdwa v121, v117 dst_sel:WORD_1 dst_unused:UNUSED_PRESERVE src0_sel:WORD_1\n"
    "v_rcp_f16_sdwa v122, v118 dst_sel:WORD_1 dst_unused:UNUSED_PRESERVE src0_sel:WORD_1\n"
    "v_rcp_f16_sdwa v123, v119 dst_sel:WORD_1 dst_unused:UNUSED_PRESERVE src0_sel:WORD_1\n"
    "v_mfma_f32_32x32x16_f16 v[36:51], v[132:135], v[108:111], v[36:51]\n"
    "ds_read_b128 v[132:135], v244 offset:29696\n"
    "v_pk_mul_f16 v112, v112, v120\n"
    "v_pk_mul_f16 v113, v113, v121\n"
    "v_pk_mul_f16 v114, v114, v122\n"
    "v_pk_mul_f16 v115, v115, v123\n"
    "v_cvt_pk_f16_f32 v108, v68, v69\n"
    "v_cvt_pk_f16_f32 v109, v70, v71\n"
    "s_waitcnt lgkmcnt(2)\n"
    "v_mfma_f32_32x32x16_f16 v[4:19], v[124:127], v[112:115], v[4:19]\n"
    "ds_read_b128 v[124:127], v244 offset:30720\n"
    "v_cvt_pk_f16_f32 v110, v72, v73\n"
    "v_cvt_pk_f16_f32 v111, v74, v75\n"
    "v_pk_fma_f16 v116, v108, v108, s57\n"
    "v_pk_fma_f16 v117, v109, v109, s57\n"
    "v_pk_fma_f16 v118, v110, v110, s57\n"
    "v_pk_fma_f16 v119, v111, v111, s57\n"
    "v_pk_mul_f16 v116, v116, v108\n"
    "v_mfma_f32_32x32x16_bf16 v[52:67], a[120:123], v[84:87], 0\n"
    "s_waitcnt lgkmcnt(2)\n"
    "v_pk_mul_f16 v117, v117, v109\n"
    "v_pk_mul_f16 v118, v118, v110\n"
    "v_pk_mul_f16 v119, v119, v111\n"
    "v_exp_f16_e32 v120, v116\n"
    "v_exp_f16_e32 v121, v117\n"
    "v_exp_f16_e32 v122, v118\n"
    "v_mfma_f32_32x32x16_f16 v[20:35], v[128:131], v[112:115], v[20:35]\n"
    "ds_read_b128 v[128:131], v244 offset:31744\n"
    "v_exp_f16_e32 v123, v119\n"
    "v_exp_f16_sdwa v120, v116 dst_sel:WORD_1 dst_unused:UNUSED_PRESERVE src0_sel:WORD_1\n"
    "v_exp_f16_sdwa v121, v117 dst_sel:WORD_1 dst_unused:UNUSED_PRESERVE src0_sel:WORD_1\n"
    "v_exp_f16_sdwa v122, v118 dst_sel:WORD_1 dst_unused:UNUSED_PRESERVE src0_sel:WORD_1\n"
    "v_mfma_f32_32x32x16_bf16 v[52:67], a[124:127], v[88:91], v[52:67]\n"
    "s_waitcnt lgkmcnt(2)\n"
    "v_exp_f16_sdwa v123, v119 dst_sel:WORD_1 dst_unused:UNUSED_PRESERVE src0_sel:WORD_1\n"
    "v_pk_fma_f16 v116, v120, s58, s58\n"
    "v_pk_fma_f16 v117, v121, s58, s58\n"
    "v_pk_fma_f16 v118, v122, s58, s58\n"
    "v_pk_fma_f16 v119, v123, s58, s58\n"
    "v_rcp_f16_e32 v120, v116\n"
    "v_mfma_f32_32x32x16_f16 v[36:51], v[132:135], v[112:115], v[36:51]\n"
    "ds_read_b128 v[132:135], v244 offset:32768\n"
    "v_rcp_f16_e32 v121, v117\n"
    "v_rcp_f16_e32 v122, v118\n"
    "v_rcp_f16_e32 v123, v119\n"
    "v_rcp_f16_sdwa v120, v116 dst_sel:WORD_1 dst_unused:UNUSED_PRESERVE src0_sel:WORD_1\n"
    "buffer_load_dwordx4 v[180:183], v253, s[36:39], s56 offen offset:128\n"
    "v_mfma_f32_32x32x16_bf16 v[52:67], a[128:131], v[92:95], v[52:67]\n"
    "v_rcp_f16_sdwa v121, v117 dst_sel:WORD_1 dst_unused:UNUSED_PRESERVE src0_sel:WORD_1\n"
    "v_rcp_f16_sdwa v122, v118 dst_sel:WORD_1 dst_unused:UNUSED_PRESERVE src0_sel:WORD_1\n"
    "v_rcp_f16_sdwa v123, v119 dst_sel:WORD_1 dst_unused:UNUSED_PRESERVE src0_sel:WORD_1\n"
    "v_pk_mul_f16 v108, v108, v120\n"
    "v_pk_mul_f16 v109, v109, v121\n"
    "v_pk_mul_f16 v110, v110, v122\n"
    "v_mfma_f32_32x32x16_bf16 v[52:67], a[132:135], v[96:99], v[52:67]\n"
    "v_pk_mul_f16 v111, v111, v123\n"
    "v_cvt_pk_f16_f32 v112, v76, v77\n"
    "v_cvt_pk_f16_f32 v113, v78, v79\n"
    "v_cvt_pk_f16_f32 v114, v80, v81\n"
    "v_cvt_pk_f16_f32 v115, v82, v83\n"
    "v_pk_fma_f16 v116, v112, v112, s57\n"
    "v_pk_fma_f16 v117, v113, v113, s57\n"
    "v_pk_fma_f16 v118, v114, v114, s57\n"
    "v_mfma_f32_32x32x16_bf16 v[52:67], a[136:139], v[100:103], v[52:67]\n"
    "v_pk_fma_f16 v119, v115, v115, s57\n"
    "v_pk_mul_f16 v116, v116, v112\n"
    "v_pk_mul_f16 v117, v117, v113\n"
    "v_pk_mul_f16 v118, v118, v114\n"
    "v_pk_mul_f16 v119, v119, v115\n"
    "v_exp_f16_e32 v120, v116\n"
    "v_exp_f16_e32 v121, v117\n"
    "v_mfma_f32_32x32x16_bf16 v[52:67], a[140:143], v[104:107], v[52:67]\n"
    "s_waitcnt lgkmcnt(2)\n"
    "v_exp_f16_e32 v122, v118\n"
    "v_exp_f16_e32 v123, v119\n"
    "v_exp_f16_sdwa v120, v116 dst_sel:WORD_1 dst_unused:UNUSED_PRESERVE src0_sel:WORD_1\n"
    "v_exp_f16_sdwa v121, v117 dst_sel:WORD_1 dst_unused:UNUSED_PRESERVE src0_sel:WORD_1\n"
    "v_exp_f16_sdwa v122, v118 dst_sel:WORD_1 dst_unused:UNUSED_PRESERVE src0_sel:WORD_1\n"
    "v_exp_f16_sdwa v123, v119 dst_sel:WORD_1 dst_unused:UNUSED_PRESERVE src0_sel:WORD_1\n"
    "v_mfma_f32_32x32x16_f16 v[4:19], v[124:127], v[108:111], v[4:19]\n"
    "s_waitcnt lgkmcnt(1)\n"
    "ds_read_b128 v[124:127], v244 offset:33792\n"
    "v_pk_fma_f16 v116, v120, s58, s58\n"
    "v_pk_fma_f16 v117, v121, s58, s58\n"
    "v_pk_fma_f16 v118, v122, s58, s58\n"
    "v_pk_fma_f16 v119, v123, s58, s58\n"
    "v_rcp_f16_e32 v120, v116\n"
    "v_rcp_f16_e32 v121, v117\n"
    "v_rcp_f16_e32 v122, v118\n"
    "v_mfma_f32_32x32x16_f16 v[20:35], v[128:131], v[108:111], v[20:35]\n"
    "s_waitcnt lgkmcnt(1)\n"
    "ds_read_b128 v[128:131], v244 offset:34816\n"
    "v_rcp_f16_e32 v123, v119\n"
    "v_rcp_f16_sdwa v120, v116 dst_sel:WORD_1 dst_unused:UNUSED_PRESERVE src0_sel:WORD_1\n"
    "v_rcp_f16_sdwa v121, v117 dst_sel:WORD_1 dst_unused:UNUSED_PRESERVE src0_sel:WORD_1\n"
    "v_rcp_f16_sdwa v122, v118 dst_sel:WORD_1 dst_unused:UNUSED_PRESERVE src0_sel:WORD_1\n"
    "v_rcp_f16_sdwa v123, v119 dst_sel:WORD_1 dst_unused:UNUSED_PRESERVE src0_sel:WORD_1\n"
    "v_mfma_f32_32x32x16_f16 v[36:51], v[132:135], v[108:111], v[36:51]\n"
    "ds_read_b128 v[132:135], v244 offset:35840\n"
    "v_pk_mul_f16 v112, v112, v120\n"
    "v_pk_mul_f16 v113, v113, v121\n"
    "v_pk_mul_f16 v114, v114, v122\n"
    "v_pk_mul_f16 v115, v115, v123\n"
    "v_cvt_pk_f16_f32 v108, v52, v53\n"
    "v_cvt_pk_f16_f32 v109, v54, v55\n"
    "s_waitcnt lgkmcnt(2)\n"
    "v_mfma_f32_32x32x16_f16 v[4:19], v[124:127], v[112:115], v[4:19]\n"
    "ds_read_b128 v[124:127], v244 offset:36864\n"
    "v_cvt_pk_f16_f32 v110, v56, v57\n"
    "v_cvt_pk_f16_f32 v111, v58, v59\n"
    "v_pk_fma_f16 v116, v108, v108, s57\n"
    "v_pk_fma_f16 v117, v109, v109, s57\n"
    "v_pk_fma_f16 v118, v110, v110, s57\n"
    "v_pk_fma_f16 v119, v111, v111, s57\n"
    "v_pk_mul_f16 v116, v116, v108\n"
    "v_mfma_f32_32x32x16_bf16 v[68:83], a[144:147], v[84:87], 0\n"
    "s_waitcnt lgkmcnt(2)\n"
    "v_pk_mul_f16 v117, v117, v109\n"
    "v_pk_mul_f16 v118, v118, v110\n"
    "v_pk_mul_f16 v119, v119, v111\n"
    "v_exp_f16_e32 v120, v116\n"
    "v_exp_f16_e32 v121, v117\n"
    "v_exp_f16_e32 v122, v118\n"
    "v_mfma_f32_32x32x16_f16 v[20:35], v[128:131], v[112:115], v[20:35]\n"
    "ds_read_b128 v[128:131], v244 offset:37888\n"
    "v_exp_f16_e32 v123, v119\n"
    "v_exp_f16_sdwa v120, v116 dst_sel:WORD_1 dst_unused:UNUSED_PRESERVE src0_sel:WORD_1\n"
    "v_exp_f16_sdwa v121, v117 dst_sel:WORD_1 dst_unused:UNUSED_PRESERVE src0_sel:WORD_1\n"
    "v_exp_f16_sdwa v122, v118 dst_sel:WORD_1 dst_unused:UNUSED_PRESERVE src0_sel:WORD_1\n"
    "v_mfma_f32_32x32x16_bf16 v[68:83], a[148:151], v[88:91], v[68:83]\n"
    "s_waitcnt lgkmcnt(2)\n"
    "v_exp_f16_sdwa v123, v119 dst_sel:WORD_1 dst_unused:UNUSED_PRESERVE src0_sel:WORD_1\n"
    "v_pk_fma_f16 v116, v120, s58, s58\n"
    "v_pk_fma_f16 v117, v121, s58, s58\n"
    "v_pk_fma_f16 v118, v122, s58, s58\n"
    "v_pk_fma_f16 v119, v123, s58, s58\n"
    "v_rcp_f16_e32 v120, v116\n"
    "v_mfma_f32_32x32x16_f16 v[36:51], v[132:135], v[112:115], v[36:51]\n"
    "ds_read_b128 v[132:135], v244 offset:38912\n"
    "v_rcp_f16_e32 v121, v117\n"
    "v_rcp_f16_e32 v122, v118\n"
    "v_rcp_f16_e32 v123, v119\n"
    "v_rcp_f16_sdwa v120, v116 dst_sel:WORD_1 dst_unused:UNUSED_PRESERVE src0_sel:WORD_1\n"
    "v_mfma_f32_32x32x16_bf16 v[68:83], a[152:155], v[92:95], v[68:83]\n"
    "v_rcp_f16_sdwa v121, v117 dst_sel:WORD_1 dst_unused:UNUSED_PRESERVE src0_sel:WORD_1\n"
    "v_rcp_f16_sdwa v122, v118 dst_sel:WORD_1 dst_unused:UNUSED_PRESERVE src0_sel:WORD_1\n"
    "v_rcp_f16_sdwa v123, v119 dst_sel:WORD_1 dst_unused:UNUSED_PRESERVE src0_sel:WORD_1\n"
    "v_pk_mul_f16 v108, v108, v120\n"
    "v_pk_mul_f16 v109, v109, v121\n"
    "v_pk_mul_f16 v110, v110, v122\n"
    "v_mfma_f32_32x32x16_bf16 v[68:83], a[156:159], v[96:99], v[68:83]\n"
    "v_pk_mul_f16 v111, v111, v123\n"
    "v_cvt_pk_f16_f32 v112, v60, v61\n"
    "v_cvt_pk_f16_f32 v113, v62, v63\n"
    "v_cvt_pk_f16_f32 v114, v64, v65\n"
    "v_cvt_pk_f16_f32 v115, v66, v67\n"
    "v_pk_fma_f16 v116, v112, v112, s57\n"
    "v_pk_fma_f16 v117, v113, v113, s57\n"
    "v_pk_fma_f16 v118, v114, v114, s57\n"
    "v_mfma_f32_32x32x16_bf16 v[68:83], a[160:163], v[100:103], v[68:83]\n"
    "v_pk_fma_f16 v119, v115, v115, s57\n"
    "v_pk_mul_f16 v116, v116, v112\n"
    "v_pk_mul_f16 v117, v117, v113\n"
    "v_pk_mul_f16 v118, v118, v114\n"
    "v_pk_mul_f16 v119, v119, v115\n"
    "v_exp_f16_e32 v120, v116\n"
    "v_exp_f16_e32 v121, v117\n"
    "v_mfma_f32_32x32x16_bf16 v[68:83], a[164:167], v[104:107], v[68:83]\n"
    "s_waitcnt lgkmcnt(2)\n"
    "v_exp_f16_e32 v122, v118\n"
    "v_exp_f16_e32 v123, v119\n"
    "v_exp_f16_sdwa v120, v116 dst_sel:WORD_1 dst_unused:UNUSED_PRESERVE src0_sel:WORD_1\n"
    "v_exp_f16_sdwa v121, v117 dst_sel:WORD_1 dst_unused:UNUSED_PRESERVE src0_sel:WORD_1\n"
    "v_exp_f16_sdwa v122, v118 dst_sel:WORD_1 dst_unused:UNUSED_PRESERVE src0_sel:WORD_1\n"
    "v_exp_f16_sdwa v123, v119 dst_sel:WORD_1 dst_unused:UNUSED_PRESERVE src0_sel:WORD_1\n"
    "v_mfma_f32_32x32x16_f16 v[4:19], v[124:127], v[108:111], v[4:19]\n"
    "s_waitcnt lgkmcnt(1)\n"
    "ds_read_b128 v[124:127], v244 offset:39936\n"
    "v_pk_fma_f16 v116, v120, s58, s58\n"
    "v_pk_fma_f16 v117, v121, s58, s58\n"
    "v_pk_fma_f16 v118, v122, s58, s58\n"
    "v_pk_fma_f16 v119, v123, s58, s58\n"
    "v_rcp_f16_e32 v120, v116\n"
    "v_rcp_f16_e32 v121, v117\n"
    "v_rcp_f16_e32 v122, v118\n"
    "v_mfma_f32_32x32x16_f16 v[20:35], v[128:131], v[108:111], v[20:35]\n"
    "s_waitcnt lgkmcnt(1)\n"
    "ds_read_b128 v[128:131], v244 offset:40960\n"
    "v_rcp_f16_e32 v123, v119\n"
    "v_rcp_f16_sdwa v120, v116 dst_sel:WORD_1 dst_unused:UNUSED_PRESERVE src0_sel:WORD_1\n"
    "v_rcp_f16_sdwa v121, v117 dst_sel:WORD_1 dst_unused:UNUSED_PRESERVE src0_sel:WORD_1\n"
    "v_rcp_f16_sdwa v122, v118 dst_sel:WORD_1 dst_unused:UNUSED_PRESERVE src0_sel:WORD_1\n"
    "v_rcp_f16_sdwa v123, v119 dst_sel:WORD_1 dst_unused:UNUSED_PRESERVE src0_sel:WORD_1\n"
    "v_mfma_f32_32x32x16_f16 v[36:51], v[132:135], v[108:111], v[36:51]\n"
    "ds_read_b128 v[132:135], v244 offset:41984\n"
    "v_pk_mul_f16 v112, v112, v120\n"
    "v_pk_mul_f16 v113, v113, v121\n"
    "v_pk_mul_f16 v114, v114, v122\n"
    "v_pk_mul_f16 v115, v115, v123\n"
    "v_cvt_pk_f16_f32 v108, v68, v69\n"
    "v_cvt_pk_f16_f32 v109, v70, v71\n"
    "s_waitcnt lgkmcnt(2)\n"
    "v_mfma_f32_32x32x16_f16 v[4:19], v[124:127], v[112:115], v[4:19]\n"
    "ds_read_b128 v[124:127], v244 offset:43008\n"
    "v_cvt_pk_f16_f32 v110, v72, v73\n"
    "v_cvt_pk_f16_f32 v111, v74, v75\n"
    "v_pk_fma_f16 v116, v108, v108, s57\n"
    "v_pk_fma_f16 v117, v109, v109, s57\n"
    "v_pk_fma_f16 v118, v110, v110, s57\n"
    "v_pk_fma_f16 v119, v111, v111, s57\n"
    "v_pk_mul_f16 v116, v116, v108\n"
    "v_mfma_f32_32x32x16_bf16 v[52:67], a[168:171], v[84:87], 0\n"
    "s_waitcnt lgkmcnt(2)\n"
    "v_pk_mul_f16 v117, v117, v109\n"
    "v_pk_mul_f16 v118, v118, v110\n"
    "v_pk_mul_f16 v119, v119, v111\n"
    "v_exp_f16_e32 v120, v116\n"
    "v_exp_f16_e32 v121, v117\n"
    "v_exp_f16_e32 v122, v118\n"
    "v_mfma_f32_32x32x16_f16 v[20:35], v[128:131], v[112:115], v[20:35]\n"
    "ds_read_b128 v[128:131], v244 offset:44032\n"
    "v_exp_f16_e32 v123, v119\n"
    "v_exp_f16_sdwa v120, v116 dst_sel:WORD_1 dst_unused:UNUSED_PRESERVE src0_sel:WORD_1\n"
    "v_exp_f16_sdwa v121, v117 dst_sel:WORD_1 dst_unused:UNUSED_PRESERVE src0_sel:WORD_1\n"
    "v_exp_f16_sdwa v122, v118 dst_sel:WORD_1 dst_unused:UNUSED_PRESERVE src0_sel:WORD_1\n"
    "v_mfma_f32_32x32x16_bf16 v[52:67], a[172:175], v[88:91], v[52:67]\n"
    "s_waitcnt lgkmcnt(2)\n"
    "v_exp_f16_sdwa v123, v119 dst_sel:WORD_1 dst_unused:UNUSED_PRESERVE src0_sel:WORD_1\n"
    "v_pk_fma_f16 v116, v120, s58, s58\n"
    "v_pk_fma_f16 v117, v121, s58, s58\n"
    "v_pk_fma_f16 v118, v122, s58, s58\n"
    "v_pk_fma_f16 v119, v123, s58, s58\n"
    "v_rcp_f16_e32 v120, v116\n"
    "v_mfma_f32_32x32x16_f16 v[36:51], v[132:135], v[112:115], v[36:51]\n"
    "ds_read_b128 v[132:135], v244 offset:45056\n"
    "v_rcp_f16_e32 v121, v117\n"
    "v_rcp_f16_e32 v122, v118\n"
    "v_rcp_f16_e32 v123, v119\n"
    "v_rcp_f16_sdwa v120, v116 dst_sel:WORD_1 dst_unused:UNUSED_PRESERVE src0_sel:WORD_1\n"
    "v_mfma_f32_32x32x16_bf16 v[52:67], a[176:179], v[92:95], v[52:67]\n"
    "v_rcp_f16_sdwa v121, v117 dst_sel:WORD_1 dst_unused:UNUSED_PRESERVE src0_sel:WORD_1\n"
    "v_rcp_f16_sdwa v122, v118 dst_sel:WORD_1 dst_unused:UNUSED_PRESERVE src0_sel:WORD_1\n"
    "v_rcp_f16_sdwa v123, v119 dst_sel:WORD_1 dst_unused:UNUSED_PRESERVE src0_sel:WORD_1\n"
    "v_pk_mul_f16 v108, v108, v120\n"
    "v_pk_mul_f16 v109, v109, v121\n"
    "v_pk_mul_f16 v110, v110, v122\n"
    "v_mfma_f32_32x32x16_bf16 v[52:67], a[180:183], v[96:99], v[52:67]\n"
    "v_pk_mul_f16 v111, v111, v123\n"
    "v_cvt_pk_f16_f32 v112, v76, v77\n"
    "v_cvt_pk_f16_f32 v113, v78, v79\n"
    "v_cvt_pk_f16_f32 v114, v80, v81\n"
    "v_cvt_pk_f16_f32 v115, v82, v83\n"
    "v_pk_fma_f16 v116, v112, v112, s57\n"
    "v_pk_fma_f16 v117, v113, v113, s57\n"
    "v_pk_fma_f16 v118, v114, v114, s57\n"
    "v_mfma_f32_32x32x16_bf16 v[52:67], a[184:187], v[100:103], v[52:67]\n"
    "v_pk_fma_f16 v119, v115, v115, s57\n"
    "v_pk_mul_f16 v116, v116, v112\n"
    "v_pk_mul_f16 v117, v117, v113\n"
    "v_pk_mul_f16 v118, v118, v114\n"
    "v_pk_mul_f16 v119, v119, v115\n"
    "v_exp_f16_e32 v120, v116\n"
    "v_exp_f16_e32 v121, v117\n"
    "v_mfma_f32_32x32x16_bf16 v[52:67], a[188:191], v[104:107], v[52:67]\n"
    "s_waitcnt lgkmcnt(2)\n"
    "v_exp_f16_e32 v122, v118\n"
    "v_exp_f16_e32 v123, v119\n"
    "v_exp_f16_sdwa v120, v116 dst_sel:WORD_1 dst_unused:UNUSED_PRESERVE src0_sel:WORD_1\n"
    "v_exp_f16_sdwa v121, v117 dst_sel:WORD_1 dst_unused:UNUSED_PRESERVE src0_sel:WORD_1\n"
    "v_exp_f16_sdwa v122, v118 dst_sel:WORD_1 dst_unused:UNUSED_PRESERVE src0_sel:WORD_1\n"
    "v_exp_f16_sdwa v123, v119 dst_sel:WORD_1 dst_unused:UNUSED_PRESERVE src0_sel:WORD_1\n"
    "v_mfma_f32_32x32x16_f16 v[4:19], v[124:127], v[108:111], v[4:19]\n"
    "s_waitcnt lgkmcnt(1)\n"
    "ds_read_b128 v[124:127], v244 offset:46080\n"
    "v_pk_fma_f16 v116, v120, s58, s58\n"
    "v_pk_fma_f16 v117, v121, s58, s58\n"
    "v_pk_fma_f16 v118, v122, s58, s58\n"
    "v_pk_fma_f16 v119, v123, s58, s58\n"
    "v_rcp_f16_e32 v120, v116\n"
    "v_rcp_f16_e32 v121, v117\n"
    "v_rcp_f16_e32 v122, v118\n"
    "v_mfma_f32_32x32x16_f16 v[20:35], v[128:131], v[108:111], v[20:35]\n"
    "s_waitcnt lgkmcnt(1)\n"
    "ds_read_b128 v[128:131], v244 offset:47104\n"
    "v_rcp_f16_e32 v123, v119\n"
    "v_rcp_f16_sdwa v120, v116 dst_sel:WORD_1 dst_unused:UNUSED_PRESERVE src0_sel:WORD_1\n"
    "v_rcp_f16_sdwa v121, v117 dst_sel:WORD_1 dst_unused:UNUSED_PRESERVE src0_sel:WORD_1\n"
    "v_rcp_f16_sdwa v122, v118 dst_sel:WORD_1 dst_unused:UNUSED_PRESERVE src0_sel:WORD_1\n"
    "v_rcp_f16_sdwa v123, v119 dst_sel:WORD_1 dst_unused:UNUSED_PRESERVE src0_sel:WORD_1\n"
    "v_mfma_f32_32x32x16_f16 v[36:51], v[132:135], v[108:111], v[36:51]\n"
    "ds_read_b128 v[132:135], v244 offset:48128\n"
    "v_pk_mul_f16 v112, v112, v120\n"
    "v_pk_mul_f16 v113, v113, v121\n"
    "v_pk_mul_f16 v114, v114, v122\n"
    "v_pk_mul_f16 v115, v115, v123\n"
    "v_cvt_pk_f16_f32 v108, v52, v53\n"
    "v_cvt_pk_f16_f32 v109, v54, v55\n"
    "s_waitcnt lgkmcnt(2)\n"
    "v_mfma_f32_32x32x16_f16 v[4:19], v[124:127], v[112:115], v[4:19]\n"
    "ds_read_b128 v[124:127], v244 offset:49152\n"
    "v_cvt_pk_f16_f32 v110, v56, v57\n"
    "v_cvt_pk_f16_f32 v111, v58, v59\n"
    "v_pk_fma_f16 v116, v108, v108, s57\n"
    "v_pk_fma_f16 v117, v109, v109, s57\n"
    "v_pk_fma_f16 v118, v110, v110, s57\n"
    "v_pk_fma_f16 v119, v111, v111, s57\n"
    "v_pk_mul_f16 v116, v116, v108\n"
    "v_mfma_f32_32x32x16_bf16 v[68:83], a[192:195], v[84:87], 0\n"
    "s_waitcnt lgkmcnt(2)\n"
    "v_pk_mul_f16 v117, v117, v109\n"
    "v_pk_mul_f16 v118, v118, v110\n"
    "v_pk_mul_f16 v119, v119, v111\n"
    "v_exp_f16_e32 v120, v116\n"
    "v_exp_f16_e32 v121, v117\n"
    "v_exp_f16_e32 v122, v118\n"
    "v_mfma_f32_32x32x16_f16 v[20:35], v[128:131], v[112:115], v[20:35]\n"
    "ds_read_b128 v[128:131], v244 offset:50176\n"
    "v_exp_f16_e32 v123, v119\n"
    "v_exp_f16_sdwa v120, v116 dst_sel:WORD_1 dst_unused:UNUSED_PRESERVE src0_sel:WORD_1\n"
    "v_exp_f16_sdwa v121, v117 dst_sel:WORD_1 dst_unused:UNUSED_PRESERVE src0_sel:WORD_1\n"
    "v_exp_f16_sdwa v122, v118 dst_sel:WORD_1 dst_unused:UNUSED_PRESERVE src0_sel:WORD_1\n"
    "v_mfma_f32_32x32x16_bf16 v[68:83], a[196:199], v[88:91], v[68:83]\n"
    "s_waitcnt lgkmcnt(2)\n"
    "v_exp_f16_sdwa v123, v119 dst_sel:WORD_1 dst_unused:UNUSED_PRESERVE src0_sel:WORD_1\n"
    "v_pk_fma_f16 v116, v120, s58, s58\n"
    "v_pk_fma_f16 v117, v121, s58, s58\n"
    "v_pk_fma_f16 v118, v122, s58, s58\n"
    "v_pk_fma_f16 v119, v123, s58, s58\n"
    "v_rcp_f16_e32 v120, v116\n"
    "v_mfma_f32_32x32x16_f16 v[36:51], v[132:135], v[112:115], v[36:51]\n"
    "ds_read_b128 v[132:135], v244 offset:51200\n"
    "v_rcp_f16_e32 v121, v117\n"
    "v_rcp_f16_e32 v122, v118\n"
    "v_rcp_f16_e32 v123, v119\n"
    "v_rcp_f16_sdwa v120, v116 dst_sel:WORD_1 dst_unused:UNUSED_PRESERVE src0_sel:WORD_1\n"
    "v_mfma_f32_32x32x16_bf16 v[68:83], a[200:203], v[92:95], v[68:83]\n"
    "v_rcp_f16_sdwa v121, v117 dst_sel:WORD_1 dst_unused:UNUSED_PRESERVE src0_sel:WORD_1\n"
    "v_rcp_f16_sdwa v122, v118 dst_sel:WORD_1 dst_unused:UNUSED_PRESERVE src0_sel:WORD_1\n"
    "v_rcp_f16_sdwa v123, v119 dst_sel:WORD_1 dst_unused:UNUSED_PRESERVE src0_sel:WORD_1\n"
    "v_pk_mul_f16 v108, v108, v120\n"
    "v_pk_mul_f16 v109, v109, v121\n"
    "v_pk_mul_f16 v110, v110, v122\n"
    "v_mfma_f32_32x32x16_bf16 v[68:83], a[204:207], v[96:99], v[68:83]\n"
    "v_pk_mul_f16 v111, v111, v123\n"
    "v_cvt_pk_f16_f32 v112, v60, v61\n"
    "v_cvt_pk_f16_f32 v113, v62, v63\n"
    "v_cvt_pk_f16_f32 v114, v64, v65\n"
    "v_cvt_pk_f16_f32 v115, v66, v67\n"
    "v_pk_fma_f16 v116, v112, v112, s57\n"
    "v_pk_fma_f16 v117, v113, v113, s57\n"
    "v_pk_fma_f16 v118, v114, v114, s57\n"
    "v_mfma_f32_32x32x16_bf16 v[68:83], a[208:211], v[100:103], v[68:83]\n"
    "v_pk_fma_f16 v119, v115, v115, s57\n"
    "v_pk_mul_f16 v116, v116, v112\n"
    "v_pk_mul_f16 v117, v117, v113\n"
    "v_pk_mul_f16 v118, v118, v114\n"
    "v_pk_mul_f16 v119, v119, v115\n"
    "v_exp_f16_e32 v120, v116\n"
    "v_exp_f16_e32 v121, v117\n"
    "v_mfma_f32_32x32x16_bf16 v[68:83], a[212:215], v[104:107], v[68:83]\n"
    "s_waitcnt lgkmcnt(2)\n"
    "v_exp_f16_e32 v122, v118\n"
    "v_exp_f16_e32 v123, v119\n"
    "v_exp_f16_sdwa v120, v116 dst_sel:WORD_1 dst_unused:UNUSED_PRESERVE src0_sel:WORD_1\n"
    "v_exp_f16_sdwa v121, v117 dst_sel:WORD_1 dst_unused:UNUSED_PRESERVE src0_sel:WORD_1\n"
    "v_exp_f16_sdwa v122, v118 dst_sel:WORD_1 dst_unused:UNUSED_PRESERVE src0_sel:WORD_1\n"
    "v_exp_f16_sdwa v123, v119 dst_sel:WORD_1 dst_unused:UNUSED_PRESERVE src0_sel:WORD_1\n"
    "v_mfma_f32_32x32x16_f16 v[4:19], v[124:127], v[108:111], v[4:19]\n"
    "s_waitcnt lgkmcnt(1)\n"
    "ds_read_b128 v[124:127], v244 offset:52224\n"
    "v_pk_fma_f16 v116, v120, s58, s58\n"
    "v_pk_fma_f16 v117, v121, s58, s58\n"
    "v_pk_fma_f16 v118, v122, s58, s58\n"
    "v_pk_fma_f16 v119, v123, s58, s58\n"
    "v_rcp_f16_e32 v120, v116\n"
    "v_rcp_f16_e32 v121, v117\n"
    "v_rcp_f16_e32 v122, v118\n"
    "buffer_load_dwordx4 v[164:167], v253, s[36:39], s56 offen\n"
    "v_mfma_f32_32x32x16_f16 v[20:35], v[128:131], v[108:111], v[20:35]\n"
    "s_waitcnt lgkmcnt(1)\n"
    "ds_read_b128 v[128:131], v244 offset:53248\n"
    "v_rcp_f16_e32 v123, v119\n"
    "v_rcp_f16_sdwa v120, v116 dst_sel:WORD_1 dst_unused:UNUSED_PRESERVE src0_sel:WORD_1\n"
    "v_rcp_f16_sdwa v121, v117 dst_sel:WORD_1 dst_unused:UNUSED_PRESERVE src0_sel:WORD_1\n"
    "v_rcp_f16_sdwa v122, v118 dst_sel:WORD_1 dst_unused:UNUSED_PRESERVE src0_sel:WORD_1\n"
    "v_rcp_f16_sdwa v123, v119 dst_sel:WORD_1 dst_unused:UNUSED_PRESERVE src0_sel:WORD_1\n"
    "v_mfma_f32_32x32x16_f16 v[36:51], v[132:135], v[108:111], v[36:51]\n"
    "s_waitcnt lgkmcnt(1)\n"
    "ds_read_b128 v[132:135], v244 offset:54272\n"
    "v_pk_mul_f16 v112, v112, v120\n"
    "v_pk_mul_f16 v113, v113, v121\n"
    "v_pk_mul_f16 v114, v114, v122\n"
    "v_pk_mul_f16 v115, v115, v123\n"
    "v_cvt_pk_f16_f32 v108, v68, v69\n"
    "v_cvt_pk_f16_f32 v109, v70, v71\n"
    "v_mfma_f32_32x32x16_f16 v[4:19], v[124:127], v[112:115], v[4:19]\n"
    "ds_read_b128 v[124:127], v244 offset:55296\n"
    "v_cvt_pk_f16_f32 v110, v72, v73\n"
    "v_cvt_pk_f16_f32 v111, v74, v75\n"
    "v_pk_fma_f16 v116, v108, v108, s57\n"
    "v_pk_fma_f16 v117, v109, v109, s57\n"
    "v_pk_fma_f16 v118, v110, v110, s57\n"
    "v_pk_fma_f16 v119, v111, v111, s57\n"
    "v_pk_mul_f16 v116, v116, v108\n"
    "v_mfma_f32_32x32x16_bf16 v[52:67], a[216:219], v[84:87], 0\n"
    "s_waitcnt lgkmcnt(2)\n"
    "v_pk_mul_f16 v117, v117, v109\n"
    "v_pk_mul_f16 v118, v118, v110\n"
    "v_pk_mul_f16 v119, v119, v111\n"
    "v_exp_f16_e32 v120, v116\n"
    "v_exp_f16_e32 v121, v117\n"
    "v_exp_f16_e32 v122, v118\n"
    "v_mfma_f32_32x32x16_f16 v[20:35], v[128:131], v[112:115], v[20:35]\n"
    "ds_read_b128 v[128:131], v244 offset:56320\n"
    "buffer_load_dwordx4 v[168:171], v253, s[36:39], s56 offen offset:32\n"
    "v_exp_f16_e32 v123, v119\n"
    "v_exp_f16_sdwa v120, v116 dst_sel:WORD_1 dst_unused:UNUSED_PRESERVE src0_sel:WORD_1\n"
    "v_exp_f16_sdwa v121, v117 dst_sel:WORD_1 dst_unused:UNUSED_PRESERVE src0_sel:WORD_1\n"
    "v_exp_f16_sdwa v122, v118 dst_sel:WORD_1 dst_unused:UNUSED_PRESERVE src0_sel:WORD_1\n"
    "v_mfma_f32_32x32x16_bf16 v[52:67], a[220:223], v[88:91], v[52:67]\n"
    "s_waitcnt lgkmcnt(2)\n"
    "v_exp_f16_sdwa v123, v119 dst_sel:WORD_1 dst_unused:UNUSED_PRESERVE src0_sel:WORD_1\n"
    "v_pk_fma_f16 v116, v120, s58, s58\n"
    "v_pk_fma_f16 v117, v121, s58, s58\n"
    "v_pk_fma_f16 v118, v122, s58, s58\n"
    "v_pk_fma_f16 v119, v123, s58, s58\n"
    "v_rcp_f16_e32 v120, v116\n"
    "v_mfma_f32_32x32x16_f16 v[36:51], v[132:135], v[112:115], v[36:51]\n"
    "ds_read_b128 v[132:135], v244 offset:57344\n"
    "v_rcp_f16_e32 v121, v117\n"
    "v_rcp_f16_e32 v122, v118\n"
    "v_rcp_f16_e32 v123, v119\n"
    "v_rcp_f16_sdwa v120, v116 dst_sel:WORD_1 dst_unused:UNUSED_PRESERVE src0_sel:WORD_1\n"
    "v_mfma_f32_32x32x16_bf16 v[52:67], a[224:227], v[92:95], v[52:67]\n"
    "v_rcp_f16_sdwa v121, v117 dst_sel:WORD_1 dst_unused:UNUSED_PRESERVE src0_sel:WORD_1\n"
    "v_rcp_f16_sdwa v122, v118 dst_sel:WORD_1 dst_unused:UNUSED_PRESERVE src0_sel:WORD_1\n"
    "v_rcp_f16_sdwa v123, v119 dst_sel:WORD_1 dst_unused:UNUSED_PRESERVE src0_sel:WORD_1\n"
    "v_pk_mul_f16 v108, v108, v120\n"
    "v_pk_mul_f16 v109, v109, v121\n"
    "v_pk_mul_f16 v110, v110, v122\n"
    "v_mfma_f32_32x32x16_bf16 v[52:67], a[228:231], v[96:99], v[52:67]\n"
    "v_pk_mul_f16 v111, v111, v123\n"
    "v_cvt_pk_f16_f32 v112, v76, v77\n"
    "v_cvt_pk_f16_f32 v113, v78, v79\n"
    "v_cvt_pk_f16_f32 v114, v80, v81\n"
    "buffer_load_dwordx4 v[172:175], v253, s[36:39], s56 offen offset:64\n"
    "v_cvt_pk_f16_f32 v115, v82, v83\n"
    "v_pk_fma_f16 v116, v112, v112, s57\n"
    "v_pk_fma_f16 v117, v113, v113, s57\n"
    "v_pk_fma_f16 v118, v114, v114, s57\n"
    "v_mfma_f32_32x32x16_bf16 v[52:67], a[232:235], v[100:103], v[52:67]\n"
    "v_pk_fma_f16 v119, v115, v115, s57\n"
    "v_pk_mul_f16 v116, v116, v112\n"
    "v_pk_mul_f16 v117, v117, v113\n"
    "v_pk_mul_f16 v118, v118, v114\n"
    "v_pk_mul_f16 v119, v119, v115\n"
    "v_exp_f16_e32 v120, v116\n"
    "v_exp_f16_e32 v121, v117\n"
    "v_mfma_f32_32x32x16_bf16 v[52:67], a[236:239], v[104:107], v[52:67]\n"
    "s_waitcnt lgkmcnt(2)\n"
    "buffer_load_dwordx4 v[176:179], v253, s[36:39], s56 offen offset:96\n"
    "v_exp_f16_e32 v122, v118\n"
    "v_exp_f16_e32 v123, v119\n"
    "v_exp_f16_sdwa v120, v116 dst_sel:WORD_1 dst_unused:UNUSED_PRESERVE src0_sel:WORD_1\n"
    "v_exp_f16_sdwa v121, v117 dst_sel:WORD_1 dst_unused:UNUSED_PRESERVE src0_sel:WORD_1\n"
    "v_exp_f16_sdwa v122, v118 dst_sel:WORD_1 dst_unused:UNUSED_PRESERVE src0_sel:WORD_1\n"
    "v_exp_f16_sdwa v123, v119 dst_sel:WORD_1 dst_unused:UNUSED_PRESERVE src0_sel:WORD_1\n"
    "v_mfma_f32_32x32x16_f16 v[4:19], v[124:127], v[108:111], v[4:19]\n"
    "s_waitcnt lgkmcnt(1)\n"
    "ds_read_b128 v[124:127], v244 offset:58368\n"
    "ds_read_b128 v[140:143], v245 offset:8192\n"
    "v_pk_fma_f16 v116, v120, s58, s58\n"
    "v_pk_fma_f16 v117, v121, s58, s58\n"
    "v_pk_fma_f16 v118, v122, s58, s58\n"
    "v_pk_fma_f16 v119, v123, s58, s58\n"
    "v_rcp_f16_e32 v120, v116\n"
    "v_rcp_f16_e32 v121, v117\n"
    "v_rcp_f16_e32 v122, v118\n"
    "ds_read_b128 v[144:147], v245 offset:9216\n"
    "v_mfma_f32_32x32x16_f16 v[20:35], v[128:131], v[108:111], v[20:35]\n"
    "s_waitcnt lgkmcnt(3)\n"
    "ds_read_b128 v[128:131], v244 offset:59392\n"
    "v_rcp_f16_e32 v123, v119\n"
    "v_rcp_f16_sdwa v120, v116 dst_sel:WORD_1 dst_unused:UNUSED_PRESERVE src0_sel:WORD_1\n"
    "v_rcp_f16_sdwa v121, v117 dst_sel:WORD_1 dst_unused:UNUSED_PRESERVE src0_sel:WORD_1\n"
    "ds_read_b128 v[148:151], v245 offset:10240\n"
    "ds_read_b128 v[152:155], v245 offset:11264\n"
    "v_rcp_f16_sdwa v122, v118 dst_sel:WORD_1 dst_unused:UNUSED_PRESERVE src0_sel:WORD_1\n"
    "v_rcp_f16_sdwa v123, v119 dst_sel:WORD_1 dst_unused:UNUSED_PRESERVE src0_sel:WORD_1\n"
    "v_mfma_f32_32x32x16_f16 v[36:51], v[132:135], v[108:111], v[36:51]\n"
    "s_waitcnt lgkmcnt(5)\n"
    "ds_read_b128 v[132:135], v244 offset:60416\n"
    "v_pk_mul_f16 v112, v112, v120\n"
    "v_pk_mul_f16 v113, v113, v121\n"
    "v_pk_mul_f16 v114, v114, v122\n"
    "v_pk_mul_f16 v115, v115, v123\n"
    "v_cvt_pk_f16_f32 v108, v52, v53\n"
    "v_cvt_pk_f16_f32 v109, v54, v55\n"
    "v_mfma_f32_32x32x16_f16 v[4:19], v[124:127], v[112:115], v[4:19]\n"
    "s_waitcnt lgkmcnt(5)\n"
    "ds_read_b128 v[124:127], v244 offset:61440\n"
    "v_cvt_pk_f16_f32 v110, v56, v57\n"
    "v_cvt_pk_f16_f32 v111, v58, v59\n"
    "v_pk_fma_f16 v116, v108, v108, s57\n"
    "v_pk_fma_f16 v117, v109, v109, s57\n"
    "v_pk_fma_f16 v118, v110, v110, s57\n"
    "v_pk_fma_f16 v119, v111, v111, s57\n"
    "v_pk_mul_f16 v116, v116, v108\n"
    "v_mfma_f32_32x32x16_bf16 v[68:83], v[140:143], v[84:87], 0\n"
    "s_waitcnt lgkmcnt(4)\n"
    "ds_read_b128 v[140:143], v245 offset:12288\n"
    "v_pk_mul_f16 v117, v117, v109\n"
    "v_pk_mul_f16 v118, v118, v110\n"
    "v_pk_mul_f16 v119, v119, v111\n"
    "v_exp_f16_e32 v120, v116\n"
    "v_exp_f16_e32 v121, v117\n"
    "buffer_load_dwordx4 v[160:163], v253, s[36:39], s56 offen offset:160\n"
    "v_exp_f16_e32 v122, v118\n"
    "v_mfma_f32_32x32x16_f16 v[20:35], v[128:131], v[112:115], v[20:35]\n"
    "ds_read_b128 v[128:131], v244 offset:62464\n"
    "v_exp_f16_e32 v123, v119\n"
    "v_exp_f16_sdwa v120, v116 dst_sel:WORD_1 dst_unused:UNUSED_PRESERVE src0_sel:WORD_1\n"
    "v_exp_f16_sdwa v121, v117 dst_sel:WORD_1 dst_unused:UNUSED_PRESERVE src0_sel:WORD_1\n"
    "v_exp_f16_sdwa v122, v118 dst_sel:WORD_1 dst_unused:UNUSED_PRESERVE src0_sel:WORD_1\n"
    "v_mfma_f32_32x32x16_bf16 v[68:83], v[144:147], v[88:91], v[68:83]\n"
    "s_waitcnt lgkmcnt(3)\n"
    "ds_read_b128 v[144:147], v245 offset:13312\n"
    "v_exp_f16_sdwa v123, v119 dst_sel:WORD_1 dst_unused:UNUSED_PRESERVE src0_sel:WORD_1\n"
    "v_pk_fma_f16 v116, v120, s58, s58\n"
    "v_pk_fma_f16 v117, v121, s58, s58\n"
    "v_pk_fma_f16 v118, v122, s58, s58\n"
    "v_pk_fma_f16 v119, v123, s58, s58\n"
    "v_rcp_f16_e32 v120, v116\n"
    "v_mfma_f32_32x32x16_f16 v[36:51], v[132:135], v[112:115], v[36:51]\n"
    "ds_read_b128 v[132:135], v244 offset:63488\n"
    "v_rcp_f16_e32 v121, v117\n"
    "v_rcp_f16_e32 v122, v118\n"
    "v_rcp_f16_e32 v123, v119\n"
    "v_rcp_f16_sdwa v120, v116 dst_sel:WORD_1 dst_unused:UNUSED_PRESERVE src0_sel:WORD_1\n"
    "v_mfma_f32_32x32x16_bf16 v[68:83], v[148:151], v[92:95], v[68:83]\n"
    "v_rcp_f16_sdwa v121, v117 dst_sel:WORD_1 dst_unused:UNUSED_PRESERVE src0_sel:WORD_1\n"
    "v_rcp_f16_sdwa v122, v118 dst_sel:WORD_1 dst_unused:UNUSED_PRESERVE src0_sel:WORD_1\n"
    "v_rcp_f16_sdwa v123, v119 dst_sel:WORD_1 dst_unused:UNUSED_PRESERVE src0_sel:WORD_1\n"
    "v_pk_mul_f16 v108, v108, v120\n"
    "v_pk_mul_f16 v109, v109, v121\n"
    "v_pk_mul_f16 v110, v110, v122\n"
    "v_mfma_f32_32x32x16_bf16 v[68:83], v[152:155], v[96:99], v[68:83]\n"
    "s_waitcnt lgkmcnt(3)\n"
    "v_pk_mul_f16 v111, v111, v123\n"
    "v_cvt_pk_f16_f32 v112, v60, v61\n"
    "v_cvt_pk_f16_f32 v113, v62, v63\n"
    "v_cvt_pk_f16_f32 v114, v64, v65\n"
    "v_cvt_pk_f16_f32 v115, v66, v67\n"
    "v_pk_fma_f16 v116, v112, v112, s57\n"
    "v_pk_fma_f16 v117, v113, v113, s57\n"
    "v_pk_fma_f16 v118, v114, v114, s57\n"
    "v_mfma_f32_32x32x16_bf16 v[68:83], v[140:143], v[100:103], v[68:83]\n"
    "s_waitcnt lgkmcnt(1)\n"
    "v_pk_fma_f16 v119, v115, v115, s57\n"
    "v_pk_mul_f16 v116, v116, v112\n"
    "v_pk_mul_f16 v117, v117, v113\n"
    "v_pk_mul_f16 v118, v118, v114\n"
    "v_pk_mul_f16 v119, v119, v115\n"
    "v_exp_f16_e32 v120, v116\n"
    "v_exp_f16_e32 v121, v117\n"
    "v_mfma_f32_32x32x16_bf16 v[68:83], v[144:147], v[104:107], v[68:83]\n"
    "v_exp_f16_e32 v122, v118\n"
    "v_exp_f16_e32 v123, v119\n"
    "v_exp_f16_sdwa v120, v116 dst_sel:WORD_1 dst_unused:UNUSED_PRESERVE src0_sel:WORD_1\n"
    "ds_read_b128 v[136:139], v245 offset:20480\n"
    "v_exp_f16_sdwa v121, v117 dst_sel:WORD_1 dst_unused:UNUSED_PRESERVE src0_sel:WORD_1\n"
    "v_exp_f16_sdwa v122, v118 dst_sel:WORD_1 dst_unused:UNUSED_PRESERVE src0_sel:WORD_1\n"
    "v_exp_f16_sdwa v123, v119 dst_sel:WORD_1 dst_unused:UNUSED_PRESERVE src0_sel:WORD_1\n"
    "v_mfma_f32_32x32x16_f16 v[4:19], v[124:127], v[108:111], v[4:19]\n"
    "ds_read_b128 v[124:127], v244 offset:64512\n"
    "v_pk_fma_f16 v116, v120, s58, s58\n"
    "v_pk_fma_f16 v117, v121, s58, s58\n"
    "v_pk_fma_f16 v118, v122, s58, s58\n"
    "v_pk_fma_f16 v119, v123, s58, s58\n"
    "ds_read_b128 v[156:159], v245 offset:21504\n"
    "v_rcp_f16_e32 v120, v116\n"
    "v_rcp_f16_e32 v121, v117\n"
    "v_rcp_f16_e32 v122, v118\n"
    "v_mfma_f32_32x32x16_f16 v[20:35], v[128:131], v[108:111], v[20:35]\n"
    "s_waitcnt lgkmcnt(3)\n"
    "ds_read_b128 v[128:131], v245\n"
    "v_rcp_f16_e32 v123, v119\n"
    "v_rcp_f16_sdwa v120, v116 dst_sel:WORD_1 dst_unused:UNUSED_PRESERVE src0_sel:WORD_1\n"
    "v_rcp_f16_sdwa v121, v117 dst_sel:WORD_1 dst_unused:UNUSED_PRESERVE src0_sel:WORD_1\n"
    "v_rcp_f16_sdwa v122, v118 dst_sel:WORD_1 dst_unused:UNUSED_PRESERVE src0_sel:WORD_1\n"
    "v_rcp_f16_sdwa v123, v119 dst_sel:WORD_1 dst_unused:UNUSED_PRESERVE src0_sel:WORD_1\n"
    "v_mfma_f32_32x32x16_f16 v[36:51], v[132:135], v[108:111], v[36:51]\n"
    "s_waitcnt lgkmcnt(2)\n"
    "ds_read_b128 v[132:135], v245 offset:1024\n"
    "v_pk_mul_f16 v112, v112, v120\n"
    "v_pk_mul_f16 v113, v113, v121\n"
    "v_pk_mul_f16 v114, v114, v122\n"
    "v_pk_mul_f16 v115, v115, v123\n"
    "v_cvt_pk_f16_f32 v108, v68, v69\n"
    "v_cvt_pk_f16_f32 v109, v70, v71\n"
    "v_mfma_f32_32x32x16_f16 v[4:19], v[124:127], v[112:115], v[4:19]\n"
    "ds_read_b128 v[124:127], v245 offset:2048\n"
    "v_cvt_pk_f16_f32 v110, v72, v73\n"
    "v_cvt_pk_f16_f32 v111, v74, v75\n"
    "v_pk_fma_f16 v116, v108, v108, s57\n"
    "v_pk_fma_f16 v117, v109, v109, s57\n"
    "v_pk_fma_f16 v118, v110, v110, s57\n"
    "v_pk_fma_f16 v119, v111, v111, s57\n"
    "v_pk_mul_f16 v116, v116, v108\n"
    "v_pk_mul_f16 v117, v117, v109\n"
    "v_pk_mul_f16 v118, v118, v110\n"
    "v_pk_mul_f16 v119, v119, v111\n"
    "v_exp_f16_e32 v120, v116\n"
    "v_exp_f16_e32 v121, v117\n"
    "v_exp_f16_e32 v122, v118\n"
    "v_exp_f16_e32 v123, v119\n"
    "v_exp_f16_sdwa v120, v116 dst_sel:WORD_1 dst_unused:UNUSED_PRESERVE src0_sel:WORD_1\n"
    "v_exp_f16_sdwa v121, v117 dst_sel:WORD_1 dst_unused:UNUSED_PRESERVE src0_sel:WORD_1\n"
    "v_exp_f16_sdwa v122, v118 dst_sel:WORD_1 dst_unused:UNUSED_PRESERVE src0_sel:WORD_1\n"
    "v_exp_f16_sdwa v123, v119 dst_sel:WORD_1 dst_unused:UNUSED_PRESERVE src0_sel:WORD_1\n"
    "v_pk_fma_f16 v116, v120, s58, s58\n"
    "v_pk_fma_f16 v117, v121, s58, s58\n"
    "v_pk_fma_f16 v118, v122, s58, s58\n"
    "v_pk_fma_f16 v119, v123, s58, s58\n"
    "v_rcp_f16_e32 v120, v116\n"
    "v_rcp_f16_e32 v121, v117\n"
    "v_rcp_f16_e32 v122, v118\n"
    "v_rcp_f16_e32 v123, v119\n"
    "v_rcp_f16_sdwa v120, v116 dst_sel:WORD_1 dst_unused:UNUSED_PRESERVE src0_sel:WORD_1\n"
    "v_rcp_f16_sdwa v121, v117 dst_sel:WORD_1 dst_unused:UNUSED_PRESERVE src0_sel:WORD_1\n"
    "v_rcp_f16_sdwa v122, v118 dst_sel:WORD_1 dst_unused:UNUSED_PRESERVE src0_sel:WORD_1\n"
    "v_rcp_f16_sdwa v123, v119 dst_sel:WORD_1 dst_unused:UNUSED_PRESERVE src0_sel:WORD_1\n"
    "v_pk_mul_f16 v108, v108, v120\n"
    "v_pk_mul_f16 v109, v109, v121\n"
    "v_pk_mul_f16 v110, v110, v122\n"
    "v_pk_mul_f16 v111, v111, v123\n"
    "s_waitcnt lgkmcnt(0)\n"
    "s_nop 0\n"
    "v_mfma_f32_32x32x16_f16 v[4:19], v[124:127], v[108:111], v[4:19]\n"
    "s_waitcnt vmcnt(4)\n"
    "ds_read_b128 v[124:127], v245 offset:5120\n"
    "v_mfma_f32_32x32x16_bf16 v[4:19], v[136:139], v[164:167], v[4:19]\n"
    "v_mfma_f32_32x32x16_f16 v[20:35], v[128:131], v[112:115], v[20:35]\n"
    "s_waitcnt vmcnt(3)\n"
    "ds_read_b128 v[128:131], v245 offset:3072\n"
    "v_mfma_f32_32x32x16_bf16 v[4:19], v[156:159], v[168:171], v[4:19]\n"
    "v_mfma_f32_32x32x16_f16 v[36:51], v[132:135], v[112:115], v[36:51]\n"
    "ds_read_b128 v[132:135], v245 offset:4096\n"
    "s_waitcnt lgkmcnt(1)\n"
    "v_mfma_f32_32x32x16_f16 v[20:35], v[128:131], v[108:111], v[20:35]\n"
    "s_waitcnt vmcnt(2)\n"
    "ds_read_b128 v[128:131], v245 offset:6144\n"
    "v_mfma_f32_32x32x16_bf16 v[20:35], v[136:139], v[172:175], v[20:35]\n"
    "s_waitcnt vmcnt(1)\n"
    "v_mfma_f32_32x32x16_bf16 v[20:35], v[156:159], v[176:179], v[20:35]\n"
    "v_cvt_pk_f16_f32 v112, v76, v77\n"
    "v_cvt_pk_f16_f32 v113, v78, v79\n"
    "s_waitcnt lgkmcnt(1)\n"
    "v_mfma_f32_32x32x16_f16 v[36:51], v[132:135], v[108:111], v[36:51]\n"
    "ds_read_b128 v[132:135], v245 offset:7168\n"
    "v_mfma_f32_32x32x16_bf16 v[36:51], v[136:139], v[180:183], v[36:51]\n"
    "s_waitcnt vmcnt(0)\n"
    "v_cvt_pk_f16_f32 v114, v80, v81\n"
    "v_cvt_pk_f16_f32 v115, v82, v83\n"
    "v_pk_fma_f16 v116, v112, v112, s57\n"
    "v_pk_fma_f16 v117, v113, v113, s57\n"
    "v_pk_fma_f16 v118, v114, v114, s57\n"
    "v_pk_fma_f16 v119, v115, v115, s57\n"
    "v_pk_mul_f16 v116, v116, v112\n"
    "v_pk_mul_f16 v117, v117, v113\n"
    "v_pk_mul_f16 v118, v118, v114\n"
    "v_pk_mul_f16 v119, v119, v115\n"
    "v_exp_f16_e32 v120, v116\n"
    "v_mfma_f32_32x32x16_bf16 v[36:51], v[156:159], v[160:163], v[36:51]\n"
    "v_exp_f16_e32 v121, v117\n"
    "v_exp_f16_e32 v122, v118\n"
    "v_exp_f16_e32 v123, v119\n"
    "v_exp_f16_sdwa v120, v116 dst_sel:WORD_1 dst_unused:UNUSED_PRESERVE src0_sel:WORD_1\n"
    "v_exp_f16_sdwa v121, v117 dst_sel:WORD_1 dst_unused:UNUSED_PRESERVE src0_sel:WORD_1\n"
    "v_exp_f16_sdwa v122, v118 dst_sel:WORD_1 dst_unused:UNUSED_PRESERVE src0_sel:WORD_1\n"
    "v_exp_f16_sdwa v123, v119 dst_sel:WORD_1 dst_unused:UNUSED_PRESERVE src0_sel:WORD_1\n"
    "v_pk_fma_f16 v116, v120, s58, s58\n"
    "v_pk_fma_f16 v117, v121, s58, s58\n"
    "v_pk_fma_f16 v118, v122, s58, s58\n"
    "v_pk_fma_f16 v119, v123, s58, s58\n"
    "v_rcp_f16_e32 v120, v116\n"
    "v_rcp_f16_e32 v121, v117\n"
    "v_rcp_f16_e32 v122, v118\n"
    "v_rcp_f16_e32 v123, v119\n"
    "v_rcp_f16_sdwa v120, v116 dst_sel:WORD_1 dst_unused:UNUSED_PRESERVE src0_sel:WORD_1\n"
    "v_rcp_f16_sdwa v121, v117 dst_sel:WORD_1 dst_unused:UNUSED_PRESERVE src0_sel:WORD_1\n"
    "v_rcp_f16_sdwa v122, v118 dst_sel:WORD_1 dst_unused:UNUSED_PRESERVE src0_sel:WORD_1\n"
    "v_rcp_f16_sdwa v123, v119 dst_sel:WORD_1 dst_unused:UNUSED_PRESERVE src0_sel:WORD_1\n"
    "v_pk_mul_f16 v112, v112, v120\n"
    "v_pk_mul_f16 v113, v113, v121\n"
    "v_pk_mul_f16 v114, v114, v122\n"
    "v_pk_mul_f16 v115, v115, v123\n"
    "s_mov_b32 s55, s56\n"
    "s_add_u32 s78, s56, 98304\n"
    "v_mfma_f32_32x32x16_f16 v[4:19], v[124:127], v[112:115], v[4:19]\n"
    "s_waitcnt lgkmcnt(1)\n"
    "v_mfma_f32_32x32x16_f16 v[20:35], v[128:131], v[112:115], v[20:35]\n"
    "s_waitcnt lgkmcnt(0)\n"
    "v_mfma_f32_32x32x16_f16 v[36:51], v[132:135], v[112:115], v[36:51]\n"
    "s_nop 7\n"
    "v_cvt_pk_bf16_f32 v4, v4, v5\n"
    "v_cvt_pk_bf16_f32 v5, v6, v7\n"
    "v_cvt_pk_bf16_f32 v6, v8, v9\n"
    "v_cvt_pk_bf16_f32 v7, v10, v11\n"
    "v_cvt_pk_bf16_f32 v12, v12, v13\n"
    "v_cvt_pk_bf16_f32 v13, v14, v15\n"
    "v_cvt_pk_bf16_f32 v14, v16, v17\n"
    "v_cvt_pk_bf16_f32 v15, v18, v19\n"
    "v_permlane16_swap_b32 v4, v12\n"
    "v_permlane16_swap_b32 v5, v13\n"
    "v_permlane16_swap_b32 v6, v14\n"
    "v_permlane16_swap_b32 v7, v15\n"
    "buffer_store_dwordx4 v[4:7], v2, s[40:43], s55 offen nt\n"
    "buffer_store_dwordx4 v[12:15], v2, s[40:43], s78 offen nt\n"
    "v_cvt_pk_bf16_f32 v20, v20, v21\n"
    "v_cvt_pk_bf16_f32 v21, v22, v23\n"
    "v_cvt_pk_bf16_f32 v22, v24, v25\n"
    "v_cvt_pk_bf16_f32 v23, v26, v27\n"
    "v_cvt_pk_bf16_f32 v28, v28, v29\n"
    "v_cvt_pk_bf16_f32 v29, v30, v31\n"
    "v_cvt_pk_bf16_f32 v30, v32, v33\n"
    "v_cvt_pk_bf16_f32 v31, v34, v35\n"
    "v_permlane16_swap_b32 v20, v28\n"
    "v_permlane16_swap_b32 v21, v29\n"
    "v_permlane16_swap_b32 v22, v30\n"
    "v_permlane16_swap_b32 v23, v31\n"
    "buffer_store_dwordx4 v[20:23], v2, s[40:43], s55 offen offset:64 nt\n"
    "buffer_store_dwordx4 v[28:31], v2, s[40:43], s78 offen offset:64 nt\n"
    "v_cvt_pk_bf16_f32 v36, v36, v37\n"
    "v_cvt_pk_bf16_f32 v37, v38, v39\n"
    "v_cvt_pk_bf16_f32 v38, v40, v41\n"
    "v_cvt_pk_bf16_f32 v39, v42, v43\n"
    "v_cvt_pk_bf16_f32 v44, v44, v45\n"
    "v_cvt_pk_bf16_f32 v45, v46, v47\n"
    "v_cvt_pk_bf16_f32 v46, v48, v49\n"
    "v_cvt_pk_bf16_f32 v47, v50, v51\n"
    "v_permlane16_swap_b32 v36, v44\n"
    "v_permlane16_swap_b32 v37, v45\n"
    "v_permlane16_swap_b32 v38, v46\n"
    "v_permlane16_swap_b32 v39, v47\n"
    "buffer_store_dwordx4 v[36:39], v2, s[40:43], s55 offen offset:128 nt\n"
    "buffer_store_dwordx4 v[44:47], v2, s[40:43], s78 offen offset:128 nt\n"
    "s_endpgm\n"
    "L_end_%=:\n"
    :
    : "s"(x), "s"(dl), "s"(ds), "s"(w1), "s"(w2), "s"(out), "s"(blockIdx.x), "v"(threadIdx.x), "s"(ldsbase)
    : "memory", "scc", "vcc", "s36","s37","s38","s39","s40","s41","s42","s43","s44","s45","s46","s47","s48","s49","s50","s51","s52","s53","s54","s55","s56","s57","s58","s59","s60","s61","s62","s63","s64","s65","s66","s67","s68","s69","s70","s71","s72","s73","s74","s75","s76","s77","s78","s79","s80","s81","s82","s83","s84","s85","s86","s87","s88","s89","s90","s91","s92","s93","s94","s95", "v1","v2","v3","v4","v5","v6","v7","v8","v9","v10","v11","v12","v13","v14","v15","v16","v17","v18","v19","v20","v21","v22","v23","v24","v25","v26","v27","v28","v29","v30","v31","v32","v33","v34","v35","v36","v37","v38","v39","v40","v41","v42","v43","v44","v45","v46","v47","v48","v49","v50","v51","v52","v53","v54","v55","v56","v57","v58","v59","v60","v61","v62","v63","v64","v65","v66","v67","v68","v69","v70","v71","v72","v73","v74","v75","v76","v77","v78","v79","v80","v81","v82","v83","v84","v85","v86","v87","v88","v89","v90","v91","v92","v93","v94","v95","v96","v97","v98","v99","v100","v101","v102","v103","v104","v105","v106","v107","v108","v109","v110","v111","v112","v113","v114","v115","v116","v117","v118","v119","v120","v121","v122","v123","v124","v125","v126","v127","v128","v129","v130","v131","v132","v133","v134","v135","v136","v137","v138","v139","v140","v141","v142","v143","v144","v145","v146","v147","v148","v149","v150","v151","v152","v153","v154","v155","v156","v157","v158","v159","v160","v161","v162","v163","v164","v165","v166","v167","v168","v169","v170","v171","v172","v173","v174","v175","v176","v177","v178","v179","v180","v181","v182","v183","v184","v185","v186","v187","v188","v189","v190","v191","v192","v193","v194","v195","v196","v197","v198","v199","v200","v201","v202","v203","v204","v205","v206","v207","v208","v209","v210","v211","v212","v213","v214","v215","v216","v217","v218","v219","v220","v221","v222","v223","v224","v225","v226","v227","v228","v229","v230","v231","v232","v233","v234","v235","v236","v237","v238","v239","v240","v241","v242","v243","v244","v245","v246","v247","v248","v249","v250","v251","v252","v253","v254","v255", "a0","a1","a2","a3","a4","a5","a6","a7","a8","a9","a10","a11","a12","a13","a14","a15","a16","a17","a18","a19","a20","a21","a22","a23","a24","a25","a26","a27","a28","a29","a30","a31","a32","a33","a34","a35","a36","a37","a38","a39","a40","a41","a42","a43","a44","a45","a46","a47","a48","a49","a50","a51","a52","a53","a54","a55","a56","a57","a58","a59","a60","a61","a62","a63","a64","a65","a66","a67","a68","a69","a70","a71","a72","a73","a74","a75","a76","a77","a78","a79","a80","a81","a82","a83","a84","a85","a86","a87","a88","a89","a90","a91","a92","a93","a94","a95","a96","a97","a98","a99","a100","a101","a102","a103","a104","a105","a106","a107","a108","a109","a110","a111","a112","a113","a114","a115","a116","a117","a118","a119","a120","a121","a122","a123","a124","a125","a126","a127","a128","a129","a130","a131","a132","a133","a134","a135","a136","a137","a138","a139","a140","a141","a142","a143","a144","a145","a146","a147","a148","a149","a150","a151","a152","a153","a154","a155","a156","a157","a158","a159","a160","a161","a162","a163","a164","a165","a166","a167","a168","a169","a170","a171","a172","a173","a174","a175","a176","a177","a178","a179","a180","a181","a182","a183","a184","a185","a186","a187","a188","a189","a190","a191","a192","a193","a194","a195","a196","a197","a198","a199","a200","a201","a202","a203","a204","a205","a206","a207","a208","a209","a210","a211","a212","a213","a214","a215","a216","a217","a218","a219","a220","a221","a222","a223","a224","a225","a226","a227","a228","a229","a230","a231","a232","a233","a234","a235","a236","a237","a238","a239","a240","a241","a242","a243","a244","a245","a246","a247","a248","a249","a250","a251","a252","a253","a254","a255");
}

extern "C" void kernel_launch(void* const* d_in, const int* in_sizes, int n_in, void* d_out, int out_size, void* d_ws, size_t ws_size,
                              hipStream_t stream) {
  standalone__modern_conv_block_replknet__vision_kernel<<<256, 256, 0, stream>>>(d_in[0], d_in[1], d_in[2], d_in[3], d_in[4], d_out);
}
```
